```python
import torch

def _softmax(x, axis=-1):
    return torch.softmax(x, dim=axis)
def _gelu(x):
    return torch.nn.functional.gelu(x, approximate="tanh")
def _silu(x):
    return torch.nn.functional.silu(x)
def _sigmoid(x):
    return torch.sigmoid(x)
def _layernorm(x, axis=-1, eps=1e-5):
    m = x.mean(dim=axis, keepdim=True)
    v = x.var(dim=axis, keepdim=True, unbiased=False)
    return (x - m) / torch.sqrt(v + eps)
def _scatter_add(tgt, idx, src, axis=0):
    return tgt.clone().index_add_(axis, idx, src)
def _segment_sum(src, seg, n):
    out = torch.zeros((n,) + src.shape[1:], dtype=src.dtype)
    return out.index_add_(0, seg, src)
def _pad(x, pw):
    flat = tuple(v for pair in reversed(pw) for v in pair)
    return torch.nn.functional.pad(x, flat)

kernel_name = 'interleave_layers__grouped_kv_mqa__glu_gated_bilinear__prompt'

def setup_inputs():

    _w = (lambda a: (lambda t: t.to(torch.bfloat16) if t.is_floating_point() else t)(torch.as_tensor(a)))
    def _A_setup():
        B, S, D, H = 1, 2048, 512, 32
        Dh = D // H
        return {
            "x": _w((lambda *s: torch.randn(*s, dtype=torch.bfloat16) * 0.05)(B, S, D)),
            "wq": _w((lambda *s: torch.randn(*s, dtype=torch.bfloat16) * 0.05)(D, H * Dh)),
            "wk": _w((lambda *s: torch.randn(*s, dtype=torch.bfloat16) * 0.05)(D, Dh)),
            "wv": _w((lambda *s: torch.randn(*s, dtype=torch.bfloat16) * 0.05)(D, Dh)),
            "wo": _w((lambda *s: torch.randn(*s, dtype=torch.bfloat16) * 0.05)(H * Dh, D)),
        }
    _A_in = _A_setup()

    def _B_setup():
        B, S, D, F = 1, 2048, 512, 1792
        return {
            "x": _w((lambda *s: torch.randn(*s, dtype=torch.bfloat16) * 0.05)(B, S, D)),
            "w_gate": _w((lambda *s: torch.randn(*s, dtype=torch.bfloat16) * 0.4)(D, F) * (D ** -0.5)),
            "w_up": _w((lambda *s: torch.randn(*s, dtype=torch.bfloat16) * 0.4)(D, F) * (D ** -0.5)),
            "w_down": _w((lambda *s: torch.randn(*s, dtype=torch.bfloat16) * 0.4)(F, D) * (F ** -0.5)),
        }
    _B_in = _B_setup()

    return {
        "A_x": _A_in["x"],
        "A_wq": _A_in["wq"],
        "A_wk": _A_in["wk"],
        "A_wv": _A_in["wv"],
        "A_wo": _A_in["wo"],
        "B_x": _B_in["x"],
        "B_w_gate": _B_in["w_gate"],
        "B_w_up": _B_in["w_up"],
        "B_w_down": _B_in["w_down"],
    }

def reference(A_x, A_wq, A_wk, A_wv, A_wo, B_x, B_w_gate, B_w_up, B_w_down):
    def _A_ref(x, wq, wk, wv, wo):
        B, S, D = x.shape
        H = 32
        Dh = D // H
        q = (lambda x, *d: x.permute(*d))((x @ wq).reshape(B, S, H, Dh), 0, 2, 1, 3)
        k = x @ wk
        v = x @ wv
        scores = torch.einsum("bhqd,bkd->bhqk", q, k) * (Dh ** -0.5)
        p = _softmax(scores, axis=-1)
        o = torch.einsum("bhqk,bkd->bhqd", p, v)
        return (lambda x, *d: x.permute(*d))(o, 0, 2, 1, 3).reshape(B, S, H * Dh) @ wo

    def _B_ref(x, w_gate, w_up, w_down):
        g = torch.einsum("bsd,df->bsf", x, w_gate)
        u = torch.einsum("bsd,df->bsf", x, w_up)
        h = g * u
        return torch.einsum("bsf,fd->bsd", h, w_down)

    x = A_x
    x = x + _A_ref(_layernorm(x, axis=-1), A_wq, A_wk, A_wv, A_wo)
    x = x + _B_ref(_layernorm(x, axis=-1), B_w_gate, B_w_up, B_w_down)
    x = x + _A_ref(_layernorm(x, axis=-1), A_wq, A_wk, A_wv, A_wo)
    x = x + _B_ref(_layernorm(x, axis=-1), B_w_gate, B_w_up, B_w_down)
    return x
```

```cpp
#include <hip/hip_runtime.h>
#include <hip/hip_bf16.h>
typedef __hip_bfloat16 bf16;
constexpr int S = 2048, D = 512, H = 32, DH = 16, F = 1792;
__device__ inline float b2f(bf16 v) { return __bfloat162float(v); }
__device__ inline bf16 f2b(float v) { return __float2bfloat16(v); }

__global__ void ln_k(const bf16* x, bf16* xn) {
  int r = blockIdx.x; int t = threadIdx.x;
  float v[8]; float s = 0;
  for (int i = 0; i < 8; i++) { v[i] = b2f(x[r * D + t * 8 + i]); s += v[i]; }
  for (int o = 32; o; o >>= 1) s += __shfl_xor(s, o);
  float m = s / D; float q = 0;
  for (int i = 0; i < 8; i++) q += (v[i] - m) * (v[i] - m);
  for (int o = 32; o; o >>= 1) q += __shfl_xor(q, o);
  float rs = rsqrtf(q / D + 1e-5f);
  for (int i = 0; i < 8; i++) xn[r * D + t * 8 + i] = f2b((v[i] - m) * rs);
}
__global__ void gemm_k(const bf16* A, const bf16* B, const bf16* R, bf16* C, int M, int N, int K) {
  int n = blockIdx.x * blockDim.x + threadIdx.x; int m = blockIdx.y;
  if (n >= N) return;
  float acc = 0;
  for (int k = 0; k < K; k++) acc += b2f(A[m * K + k]) * b2f(B[k * N + n]);
  if (R) acc = b2f(f2b(acc)) + b2f(R[m * N + n]);
  C[m * N + n] = f2b(acc);
}
__global__ void mul_k(const bf16* g, const bf16* u, bf16* h, int n) {
  int i = blockIdx.x * blockDim.x + threadIdx.x;
  if (i < n) h[i] = f2b(b2f(g[i]) * b2f(u[i]));
}
__global__ void attn_k(const bf16* q, const bf16* k, const bf16* v, bf16* o) {
  int idx = blockIdx.x * blockDim.x + threadIdx.x;
  float qq[DH]; for (int d = 0; d < DH; d++) qq[d] = b2f(q[idx * DH + d]);
  float m = -1e30f, l = 0, acc[DH]; for (int d = 0; d < DH; d++) acc[d] = 0;
  for (int j = 0; j < S; j++) {
    float s = 0;
    for (int d = 0; d < DH; d++) s += qq[d] * b2f(k[j * DH + d]);
    s *= 0.25f;
    float mn = fmaxf(m, s); float a = __expf(m - mn); float p = __expf(s - mn);
    l = l * a + p;
    for (int d = 0; d < DH; d++) acc[d] = acc[d] * a + p * b2f(v[j * DH + d]);
    m = mn;
  }
  for (int d = 0; d < DH; d++) o[idx * DH + d] = f2b(acc[d] / l);
}

extern "C" void kernel_launch(void* const* d_in, const int* in_sizes, int n_in,
                              void* d_out, int out_size, void* d_ws, size_t ws_size,
                              hipStream_t stream) {
  const bf16* x = (const bf16*)d_in[0];
  const bf16* wq = (const bf16*)d_in[1];
  const bf16* wk = (const bf16*)d_in[2];
  const bf16* wv = (const bf16*)d_in[3];
  const bf16* wo = (const bf16*)d_in[4];
  const bf16* wg = (const bf16*)d_in[6];
  const bf16* wu = (const bf16*)d_in[7];
  const bf16* wd = (const bf16*)d_in[8];
  bf16* ws = (bf16*)d_ws;
  bf16* xn = ws; bf16* q = xn + S * D; bf16* k = q + S * D; bf16* v = k + S * DH; bf16* o = v + S * DH;
  bf16* x1 = o + S * D; bf16* x2 = x1 + S * D; bf16* g = x2 + S * D; bf16* u = g + S * F; bf16* h = u + S * F;
  const bf16* xin = x;
  for (int l = 0; l < 2; l++) {
    ln_k<<<S, 64, 0, stream>>>(xin, xn);
    gemm_k<<<dim3(2, S), 256, 0, stream>>>(xn, wq, nullptr, q, S, D, D);
    gemm_k<<<dim3(1, S), 64, 0, stream>>>(xn, wk, nullptr, k, S, DH, D);
    gemm_k<<<dim3(1, S), 64, 0, stream>>>(xn, wv, nullptr, v, S, DH, D);
    attn_k<<<S * H / 64, 64, 0, stream>>>(q, k, v, o);
    gemm_k<<<dim3(2, S), 256, 0, stream>>>(o, wo, xin, x1, S, D, D);
    ln_k<<<S, 64, 0, stream>>>(x1, xn);
    gemm_k<<<dim3(7, S), 256, 0, stream>>>(xn, wg, nullptr, g, S, F, D);
    gemm_k<<<dim3(7, S), 256, 0, stream>>>(xn, wu, nullptr, u, S, F, D);
    mul_k<<<S * F / 256, 256, 0, stream>>>(g, u, h, S * F);
    bf16* xo = (l == 0) ? x2 : (bf16*)d_out;
    gemm_k<<<dim3(2, S), 256, 0, stream>>>(h, wd, x1, xo, S, D, F);
    xin = x2;
  }
}
```

```cpp
#include <hip/hip_runtime.h>
#include <hip/hip_bf16.h>
#include <stdint.h>

typedef unsigned short u16;
typedef __attribute__((ext_vector_type(2))) __bf16 bf16x2;
typedef __attribute__((ext_vector_type(8))) __bf16 bf16x8;
typedef __attribute__((ext_vector_type(16))) __bf16 bf16x16;
typedef __attribute__((ext_vector_type(2))) float f32x2;
typedef __attribute__((ext_vector_type(4))) float f32x4;
typedef __attribute__((ext_vector_type(8))) float f32x8;
typedef __attribute__((ext_vector_type(16))) float f32x16;
typedef __attribute__((ext_vector_type(2))) unsigned u32x2;
typedef __attribute__((ext_vector_type(4))) unsigned u32x4;
typedef __attribute__((ext_vector_type(4))) short s16x4;
#define LAS __attribute__((address_space(3)))
#define DEV __device__ __forceinline__

constexpr int S = 2048, D = 512, F = 1792;
constexpr float QSCALE = 0.25f * 1.4426950408889634f;

constexpr size_t WS_QF  = 0;
constexpr size_t WS_K   = WS_QF + 2097152;
constexpr size_t WS_V   = WS_K + 65536;
constexpr size_t WS_QN  = WS_V + 65536;
constexpr size_t WS_KN  = WS_QN + 4096;
constexpr size_t WS_X1  = WS_KN + 4096;
constexpr size_t WS_XN1 = WS_X1 + 2097152;
constexpr size_t WS_X2  = WS_XN1 + 2097152;
constexpr size_t WS_H   = WS_X2 + 2097152;
constexpr size_t WS_WO  = WS_H + 7340032;
constexpr size_t WS_WGU = WS_WO + 524288;
constexpr size_t WS_WD  = WS_WGU + 3670016;
constexpr size_t WS_END = WS_WD + 1835008;

DEV unsigned pack2(float lo, float hi) { f32x2 v = {lo, hi}; return __builtin_bit_cast(unsigned, __builtin_convertvector(v, bf16x2)); }
DEV float bflo(unsigned u) { return __builtin_bit_cast(float, u << 16); }
DEV float bfhi(unsigned u) { return __builtin_bit_cast(float, u & 0xffff0000u); }
DEV float rbf(float v) { return bflo(pack2(v, v)); }
DEV f32x16 mfma32(u32x4 a, u32x4 b, f32x16 c) { return __builtin_amdgcn_mfma_f32_32x32x16_bf16(__builtin_bit_cast(bf16x8, a), __builtin_bit_cast(bf16x8, b), c, 0, 0, 0); }
DEV f32x4 mfma16(u32x4 a, u32x4 b, f32x4 c) { return __builtin_amdgcn_mfma_f32_16x16x32_bf16(__builtin_bit_cast(bf16x8, a), __builtin_bit_cast(bf16x8, b), c, 0, 0, 0); }
template <int CTRL> DEV float dppf(float v) { return __builtin_bit_cast(float, __builtin_amdgcn_update_dpp(0, __builtin_bit_cast(int, v), CTRL, 0xF, 0xF, true)); }
DEV void swap32(float v, float& a, float& b) { unsigned u = __float_as_uint(v); auto r = __builtin_amdgcn_permlane32_swap(u, u, false, false); unsigned r0 = r[0], r1 = r[1]; a = __uint_as_float(r0); b = __uint_as_float(r1); }
DEV void swap16(float v, float& a, float& b) { unsigned u = __float_as_uint(v); auto r = __builtin_amdgcn_permlane16_swap(u, u, false, false); unsigned r0 = r[0], r1 = r[1]; a = __uint_as_float(r0); b = __uint_as_float(r1); }
DEV float add32(float v) { float a, b; swap32(v, a, b); return a + b; }
DEV float max32(float v) { float a, b; swap32(v, a, b); return fmaxf(a, b); }
DEV float add16(float v) { float a, b; swap16(v, a, b); return a + b; }
DEV float max16(float v) { float a, b; swap16(v, a, b); return fmaxf(a, b); }
DEV float wave_max(float v) {
  v = fmaxf(v, dppf<0xB1>(v)); v = fmaxf(v, dppf<0x4E>(v)); v = fmaxf(v, dppf<0x141>(v)); v = fmaxf(v, dppf<0x140>(v));
  v = max16(v); v = max32(v); return v; }
DEV void glds16(const void* g, unsigned lds_base) {
  unsigned sv; asm volatile("s_mov_b32 %0, m0\n\ts_mov_b32 m0, %2\n\ts_nop 0\n\tglobal_load_lds_dwordx4 %1, off\n\ts_mov_b32 m0, %0" : "=&s"(sv) : "v"(g), "s"(lds_base) : "memory"); }
DEV u32x4 lds128(const char* p) { return *(const LAS u32x4*)(p); }
DEV u32x2 ldstr(const char* p) { return __builtin_bit_cast(u32x2, __builtin_amdgcn_ds_read_tr16_b64_v4i16((LAS s16x4*)(p))); }
DEV int dperm(int hh, int j) { return (j & 3) + 8 * (j >> 2) + 4 * hh; }
DEV void ln_scalars(float sum, float sq, float& mean_b, float& rinv) {
  float mean = sum * (1.0f / D);
  float var = fmaxf(sq * (1.0f / D) - mean * mean, 0.0f);
  mean_b = rbf(mean);
  float sd = rbf(__builtin_sqrtf(rbf(rbf(var) + 1e-5f)));
  rinv = 1.0f / sd;
}

__global__ void __launch_bounds__(256) k_prep(const u16* __restrict__ wo, const u16* __restrict__ wg, const u16* __restrict__ wu, const u16* __restrict__ wd, char* __restrict__ ws) {
  const int l = threadIdx.x & 63;
  const int f = blockIdx.x * 4 + (threadIdx.x >> 6);
  const u16* src; int stride; bool perm; int hh; char* dst;
  if (f < 512) {
    int nt = f & 3, kk = (f >> 2) & 1, w = (f >> 3) & 7, t = f >> 6, m = l & 15, kg = l >> 4;
    src = wo + (size_t)((4 * t + 2 * kk + (kg >> 1)) * 16) * D + 64 * w + 16 * (m >> 2) + 4 * nt + (m & 3);
    stride = D; perm = true; hh = kg & 1; dst = ws + WS_WO + (size_t)f * 1024;
  } else if (f < 512 + 3584) {
    int g = f - 512, fb = (g & 3) + 4 * (g >> 7), ks = (g >> 2) & 31, r = l & 31;
    src = ((r < 16) ? wg : wu) + (size_t)(16 * ks) * F + 16 * fb + (r & 15);
    stride = F; perm = false; hh = l >> 5; dst = ws + WS_WGU + (size_t)(fb * 32 + ks) * 1024;
  } else {
    int g = f - 4096, ct = g & 15, fb = g >> 4, m = l & 31;
    src = wd + (size_t)(16 * fb) * D + 32 * ct + 16 * ((m >> 2) & 1) + (m & 3) + 4 * (m >> 3);
    stride = D; perm = true; hh = l >> 5; dst = ws + WS_WD + (size_t)(ct * 112 + fb) * 1024;
  }
  unsigned v[8];
#pragma unroll
  for (int j = 0; j < 8; j++) v[j] = src[(size_t)(perm ? dperm(hh, j) : 8 * hh + j) * stride];
  u32x4 o = {v[0] | (v[1] << 16), v[2] | (v[3] << 16), v[4] | (v[5] << 16), v[6] | (v[7] << 16)};
  *(u32x4*)(dst + l * 16) = o;
}

__global__ void __launch_bounds__(512, 2) k_qkv(const u16* __restrict__ x, const u16* __restrict__ wq, const u16* __restrict__ wk, const u16* __restrict__ wv, char* __restrict__ ws) {
  __shared__ __attribute__((aligned(16))) char sm[147456];
  char* XF = sm; char* W = sm + 65536; char* WKV = sm + 131072;
  const int tid = threadIdx.x, l = tid & 63, w = __builtin_amdgcn_readfirstlane(tid >> 6);
  const int b = blockIdx.x, rb = (b & 7) * 4 + (b >> 6), cb = (b >> 3) & 7;
  const unsigned lds0 = (unsigned)(uintptr_t)(LAS char*)sm;
  const int row = 8 * w + (l >> 3), c = l & 7;
  const u16* xp = x + (size_t)(rb * 64 + row) * D + c * 8;
  u32x4 xv[8];
#pragma unroll
  for (int i = 0; i < 8; i++) asm volatile("global_load_dwordx4 %0, %1, off offset:%2" : "=v"(xv[i]) : "v"(xp), "i"(i * 128) : "memory");
  {
    const int kr = l >> 3;
    const u16* wp = wq + (size_t)(w * 8 + kr) * D + cb * 64 + (c ^ (4 * ((kr >> 1) & 1))) * 8;
#pragma unroll
    for (int i = 0; i < 8; i++) glds16(wp + (size_t)i * 64 * D, lds0 + 65536 + (i * 8 + w) * 1024);
    const u16* kp = ((cb & 2) ? wv : wk) + w * 512 + l * 8;
#pragma unroll
    for (int i = 0; i < 2; i++) glds16(kp + i * 4096, lds0 + 131072 + (i * 8 + w) * 1024);
  }
  asm volatile("s_waitcnt vmcnt(10)" : "+v"(xv[0]), "+v"(xv[1]), "+v"(xv[2]), "+v"(xv[3]), "+v"(xv[4]), "+v"(xv[5]), "+v"(xv[6]), "+v"(xv[7]) :: "memory");
  float sum = 0.f, sq = 0.f;
#pragma unroll
  for (int i = 0; i < 8; i++)
#pragma unroll
    for (int e = 0; e < 4; e++) { float a = bflo(xv[i][e]), bb = bfhi(xv[i][e]); sum += a + bb; sq = fmaf(a, a, sq); sq = fmaf(bb, bb, sq); }
  sum += dppf<0xB1>(sum); sq += dppf<0xB1>(sq); sum += dppf<0x4E>(sum); sq += dppf<0x4E>(sq); sum += dppf<0x141>(sum); sq += dppf<0x141>(sq);
  float mean, rinv; ln_scalars(sum, sq, mean, rinv);
  const float nb = -mean * rinv;
  {
    char* dst = XF + (row >> 5) * 32768 + c * 512 + (((row & 31) ^ c) * 16);
#pragma unroll
    for (int i = 0; i < 8; i++) {
      u32x4 o;
#pragma unroll
      for (int e = 0; e < 4; e++) o[e] = pack2(fmaf(bflo(xv[i][e]), rinv, nb), fmaf(bfhi(xv[i][e]), rinv, nb));
      *(LAS u32x4*)(dst + i * 4096) = o;
    }
  }
  asm volatile("s_waitcnt vmcnt(0) lgkmcnt(0)\n\ts_barrier" ::: "memory");
  if (w < 4) {
    const int tt = w & 1, nt = w >> 1, hh = l >> 5, q = (l & 15) >> 2, p = l & 3;
    const char* wp = W + (8 * hh + q) * 128 + (((4 * nt + 2 * ((l >> 4) & 1) + (p >> 1)) ^ (4 * (q >> 1))) * 16) + 8 * (p & 1);
    const char* xq = XF + tt * 32768 + hh * 512;
    f32x16 acc = {};
#pragma unroll
    for (int ks = 0; ks < 32; ks++) {
      u32x2 w0 = ldstr(wp + ks * 2048), w1 = ldstr(wp + ks * 2048 + 512);
      u32x4 wf = {w0[0], w0[1], w1[0], w1[1]};
      u32x4 xf = lds128(xq + ks * 1024 + (((l & 31) ^ (((ks & 3) << 1) | hh)) * 16));
      acc = mfma32(wf, xf, acc);
    }
    const int tok = rb * 64 + tt * 32 + (l & 31);
    float n0 = 0.f, n1 = 0.f; u32x4 o0, o1;
#pragma unroll
    for (int e = 0; e < 4; e++) {
      float a0 = acc[2 * e] * QSCALE, a1 = acc[2 * e + 1] * QSCALE, b0 = acc[8 + 2 * e] * QSCALE, b1 = acc[9 + 2 * e] * QSCALE;
      n0 = fmaf(a0, a0, fmaf(a1, a1, n0)); n1 = fmaf(b0, b0, fmaf(b1, b1, n1));
      o0[e] = pack2(a0, a1); o1[e] = pack2(b0, b1);
    }
    char* qd = ws + WS_QF + (size_t)((tok >> 3) * 8 + cb) * 1024 + (hh * 32 + 2 * nt * 8 + (tok & 7)) * 16;
    *(u32x4*)qd = o0; *(u32x4*)(qd + 128) = o1;
    float nm = wave_max(fmaxf(add32(n0), add32(n1)));
    if (l == 0) ((float*)(ws + WS_QN))[(rb * 2 + tt) * 16 + cb * 2 + nt] = nm;
  } else if (w < 6 && cb < 4) {
    const int j = w - 4, tt = cb & 1, kg = l >> 4, q = (l & 15) >> 2, p = l & 3;
    const char* wp = WKV + (8 * kg + q) * 32 + 8 * p;
    const char* xq = XF + tt * 32768 + kg * 512;
    f32x4 acc = {};
    const bool isV = (cb & 2) != 0;
#pragma unroll
    for (int kk = 0; kk < 16; kk++) {
      u32x2 w0 = ldstr(wp + kk * 1024), w1 = ldstr(wp + kk * 1024 + 128);
      u32x4 wf = {w0[0], w0[1], w1[0], w1[1]};
      u32x4 xf = lds128(xq + kk * 2048 + (((16 * j + (l & 15)) ^ ((kk & 1) * 4 + kg)) * 16));
      acc = isV ? mfma16(xf, wf, acc) : mfma16(wf, xf, acc);
    }
    const int T = rb * 2 + tt;
    u32x2 o = {pack2(acc[0], acc[1]), pack2(acc[2], acc[3])};
    if (isV) {
      *(u32x2*)(ws + WS_V + (size_t)(((T * 2 + j) * 2 + (kg >> 1)) * 128 + (l & 15) * 8 + 4 * (kg & 1)) * 2) = o;
    } else {
      *(u32x2*)(ws + WS_K + (size_t)((T * 32 + 16 * j + (l & 15)) * 16 + (kg & 1) * 8 + 4 * (kg >> 1)) * 2) = o;
      float n = acc[0] * acc[0] + acc[1] * acc[1] + acc[2] * acc[2] + acc[3] * acc[3];
      n = wave_max(add32(add16(n)));
      if (l == 0) ((float*)(ws + WS_KN))[T * 2 + j] = n;
    }
  }
}

constexpr int PART = 2304;
constexpr int B_OF = 64 * PART;
constexpr int B_ST = B_OF + 8192;
constexpr int B_FL = B_ST + 512;
constexpr int B_LDS = B_FL + 64;

DEV void lds_wait_eq(const char* p, unsigned v) {
  while (true) {
    unsigned x = __builtin_amdgcn_readfirstlane(*(volatile const LAS unsigned*)p);
    if (x == v) break;
    __builtin_amdgcn_s_sleep(1);
  }
  asm volatile("" ::: "memory");
}

template <bool TRUEMAX>
DEV void attn_reduce(char* sm, int t, int l) {
  lds_wait_eq(sm + B_FL + t * 4, 8u);
  const char* pp = sm + t * 8 * PART;
  f32x4 a0 = {}, a1 = {}; float ls = 0.f;
  float mw[8], mm = 0.f;
  if (TRUEMAX) {
#pragma unroll
    for (int ww = 0; ww < 8; ww++) mw[ww] = *(const LAS float*)(pp + ww * PART + 2048 + 128 + (l & 31) * 4);
    mm = mw[0];
#pragma unroll
    for (int ww = 1; ww < 8; ww++) mm = fmaxf(mm, mw[ww]);
  }
#pragma unroll
  for (int ww = 0; ww < 8; ww++) {
    f32x4 p0 = *(const LAS f32x4*)(pp + ww * PART + l * 16), p1 = *(const LAS f32x4*)(pp + ww * PART + 1024 + l * 16);
    float lv = *(const LAS float*)(pp + ww * PART + 2048 + (l & 31) * 4);
    if (TRUEMAX) { float f = __builtin_amdgcn_exp2f(mw[ww] - mm); p0 *= f; p1 *= f; lv *= f; }
    a0 += p0; a1 += p1; ls += lv;
  }
  const float inv = __builtin_amdgcn_rcpf(ls);
  u32x4 o = {pack2(a0[0] * inv, a0[1] * inv), pack2(a0[2] * inv, a0[3] * inv), pack2(a1[0] * inv, a1[1] * inv), pack2(a1[2] * inv, a1[3] * inv)};
  *(LAS u32x4*)(sm + B_OF + t * 1024 + l * 16) = o;
  asm volatile("s_waitcnt lgkmcnt(0)" ::: "memory");
  if (l == 0) *(volatile LAS unsigned*)(sm + B_FL + 32 + t * 4) = 1u;
}

DEV void attn_oproj(char* sm, int t, int l, const u32x4 (&wf)[8], f32x4 (&acc)[4]) {
  lds_wait_eq(sm + B_FL + 32 + t * 4, 1u);
  const int kg = l >> 4;
  const char* op = sm + B_OF + t * 1024 + ((kg & 1) * 32 + (kg >> 1) * 8 + (l & 7)) * 16;
  u32x4 of0 = lds128(op), of1 = lds128(op + 256);
#pragma unroll
  for (int nt = 0; nt < 4; nt++) { acc[nt] = mfma16(wf[nt], of0, acc[nt]); }
#pragma unroll
  for (int nt = 0; nt < 4; nt++) { acc[nt] = mfma16(wf[4 + nt], of1, acc[nt]); }
}

#define ATTN_CORE_1 \
  "v_mfma_f32_32x32x8_bf16 %[o], %[z], %[z], 0\n\t" \
  "v_exp_f32 v224, v224\n\t" \
  "v_exp_f32 v225, v225\n\t" \
  "v_exp_f32 v226, v226\n\t" \
  "v_exp_f32 v227, v227\n\t" \
  "v_exp_f32 v228, v228\n\t" \
  "v_mfma_f32_32x32x16_bf16 v[240:255], %[k1], %[q], %[cin]\n\t" \
  "v_exp_f32 v229, v229\n\t" \
  "v_exp_f32 v230, v230\n\t" \
  "v_exp_f32 v231, v231\n\t" \
  "v_exp_f32 v232, v232\n\t" \
  "v_exp_f32 v233, v233\n\t" \
  "v_smfmac_f32_32x32x32_bf16 %[o], %[v0], v[224:231], %[idx]\n\t" \
  "v_exp_f32 v234, v234\n\t" \
  "v_exp_f32 v235, v235\n\t" \
  "v_exp_f32 v236, v236\n\t" \
  "v_exp_f32 v237, v237\n\t" \
  "v_exp_f32 v238, v238\n\t" \
  "v_exp_f32 v239, v239\n\t" \
  "v_exp_f32 v240, v240\n\t" \
  "v_exp_f32 v241, v241\n\t" \
  "v_smfmac_f32_32x32x32_bf16 %[o], %[v1], v[232:239], %[idx]\n\t" \
  "v_exp_f32 v242, v242\n\t" \
  "v_exp_f32 v243, v243\n\t" \
  "v_exp_f32 v244, v244\n\t" \
  "v_mfma_f32_32x32x16_bf16 v[224:239], %[k2], %[q], %[cin]\n\t" \
  "v_exp_f32 v245, v245\n\t" \
  "v_exp_f32 v246, v246\n\t" \
  "v_exp_f32 v247, v247\n\t" \
  "v_exp_f32 v248, v248\n\t" \
  "v_exp_f32 v249, v249\n\t" \
  "v_smfmac_f32_32x32x32_bf16 %[o], %[v2], v[240:247], %[idx]\n\t" \
  "v_exp_f32 v250, v250\n\t" \
  "v_exp_f32 v251, v251\n\t" \
  "v_exp_f32 v252, v252\n\t" \
  "v_exp_f32 v253, v253\n\t" \
  "v_exp_f32 v254, v254\n\t" \
  "v_exp_f32 v255, v255\n\t" \
  "v_exp_f32 v224, v224\n\t" \
  "v_exp_f32 v225, v225\n\t" \
  "v_smfmac_f32_32x32x32_bf16 %[o], %[v3], v[248:255], %[idx]\n\t" \
  "v_exp_f32 v226, v226\n\t" \
  "v_exp_f32 v227, v227\n\t" \
  "v_exp_f32 v228, v228\n\t" \
  "v_mfma_f32_32x32x16_bf16 v[240:255], %[k3], %[q], %[cin]\n\t" \
  "v_exp_f32 v229, v229\n\t" \
  "v_exp_f32 v230, v230\n\t" \
  "v_exp_f32 v231, v231\n\t" \
  "v_exp_f32 v232, v232\n\t" \
  "v_exp_f32 v233, v233\n\t" \
  "v_smfmac_f32_32x32x32_bf16 %[o], %[v4], v[224:231], %[idx]\n\t" \
  "v_exp_f32 v234, v234\n\t" \
  "v_exp_f32 v235, v235\n\t" \
  "v_exp_f32 v236, v236\n\t" \
  "v_exp_f32 v237, v237\n\t" \
  "v_exp_f32 v238, v238\n\t" \
  "v_exp_f32 v239, v239\n\t" \
  "v_exp_f32 v240, v240\n\t" \
  "v_exp_f32 v241, v241\n\t" \
  "v_smfmac_f32_32x32x32_bf16 %[o], %[v5], v[232:239], %[idx]\n\t" \
  "v_exp_f32 v242, v242\n\t" \
  "v_exp_f32 v243, v243\n\t" \
  "v_exp_f32 v244, v244\n\t" \
  "v_mfma_f32_32x32x16_bf16 v[224:239], %[k4], %[q], %[cin]\n\t" \
  "v_exp_f32 v245, v245\n\t" \
  "v_exp_f32 v246, v246\n\t" \
  "v_exp_f32 v247, v247\n\t" \
  "v_exp_f32 v248, v248\n\t" \
  "v_exp_f32 v249, v249\n\t" \
  "v_smfmac_f32_32x32x32_bf16 %[o], %[v6], v[240:247], %[idx]\n\t" \
  "v_exp_f32 v250, v250\n\t" \
  "v_exp_f32 v251, v251\n\t" \
  "v_exp_f32 v252, v252\n\t" \
  "v_exp_f32 v253, v253\n\t" \
  "v_exp_f32 v254, v254\n\t" \
  "v_exp_f32 v255, v255\n\t"

#define ATTN_CORE_2 \
  "v_exp_f32 v224, v224\n\t" \
  "v_exp_f32 v225, v225\n\t" \
  "v_smfmac_f32_32x32x32_bf16 %[o], %[v7], v[248:255], %[idx]\n\t" \
  "v_exp_f32 v226, v226\n\t" \
  "v_exp_f32 v227, v227\n\t" \
  "v_exp_f32 v228, v228\n\t" \
  "v_mfma_f32_32x32x16_bf16 v[240:255], %[k5], %[q], %[cin]\n\t" \
  "v_exp_f32 v229, v229\n\t" \
  "v_exp_f32 v230, v230\n\t" \
  "v_exp_f32 v231, v231\n\t" \
  "v_exp_f32 v232, v232\n\t" \
  "v_exp_f32 v233, v233\n\t" \
  "v_smfmac_f32_32x32x32_bf16 %[o], %[v8], v[224:231], %[idx]\n\t" \
  "v_exp_f32 v234, v234\n\t" \
  "v_exp_f32 v235, v235\n\t" \
  "v_exp_f32 v236, v236\n\t" \
  "v_exp_f32 v237, v237\n\t" \
  "v_exp_f32 v238, v238\n\t" \
  "v_exp_f32 v239, v239\n\t" \
  "v_exp_f32 v240, v240\n\t" \
  "v_exp_f32 v241, v241\n\t" \
  "v_smfmac_f32_32x32x32_bf16 %[o], %[v9], v[232:239], %[idx]\n\t" \
  "v_exp_f32 v242, v242\n\t" \
  "v_exp_f32 v243, v243\n\t" \
  "v_exp_f32 v244, v244\n\t" \
  "v_mfma_f32_32x32x16_bf16 v[224:239], %[k6], %[q], %[cin]\n\t" \
  "v_exp_f32 v245, v245\n\t" \
  "v_exp_f32 v246, v246\n\t" \
  "v_exp_f32 v247, v247\n\t" \
  "v_exp_f32 v248, v248\n\t" \
  "v_exp_f32 v249, v249\n\t" \
  "v_smfmac_f32_32x32x32_bf16 %[o], %[v10], v[240:247], %[idx]\n\t" \
  "v_exp_f32 v250, v250\n\t" \
  "v_exp_f32 v251, v251\n\t" \
  "v_exp_f32 v252, v252\n\t" \
  "v_exp_f32 v253, v253\n\t" \
  "v_exp_f32 v254, v254\n\t" \
  "v_exp_f32 v255, v255\n\t" \
  "v_exp_f32 v224, v224\n\t" \
  "v_exp_f32 v225, v225\n\t" \
  "v_smfmac_f32_32x32x32_bf16 %[o], %[v11], v[248:255], %[idx]\n\t" \
  "v_exp_f32 v226, v226\n\t" \
  "v_exp_f32 v227, v227\n\t" \
  "v_exp_f32 v228, v228\n\t" \
  "v_mfma_f32_32x32x16_bf16 v[240:255], %[k7], %[q], %[cin]\n\t" \
  "v_exp_f32 v229, v229\n\t" \
  "v_exp_f32 v230, v230\n\t" \
  "v_exp_f32 v231, v231\n\t" \
  "v_exp_f32 v232, v232\n\t" \
  "v_exp_f32 v233, v233\n\t" \
  "v_smfmac_f32_32x32x32_bf16 %[o], %[v12], v[224:231], %[idx]\n\t" \
  "v_exp_f32 v234, v234\n\t" \
  "v_exp_f32 v235, v235\n\t" \
  "v_exp_f32 v236, v236\n\t" \
  "v_exp_f32 v237, v237\n\t" \
  "v_exp_f32 v238, v238\n\t" \
  "v_exp_f32 v239, v239\n\t" \
  "v_exp_f32 v240, v240\n\t" \
  "v_exp_f32 v241, v241\n\t" \
  "v_smfmac_f32_32x32x32_bf16 %[o], %[v13], v[232:239], %[idx]\n\t" \
  "v_exp_f32 v242, v242\n\t" \
  "v_exp_f32 v243, v243\n\t" \
  "v_exp_f32 v244, v244\n\t" \
  "v_mfma_f32_32x32x16_bf16 v[224:239], %[k0], %[qn], %[cin]\n\t" \
  "v_exp_f32 v245, v245\n\t" \
  "v_exp_f32 v246, v246\n\t" \
  "v_exp_f32 v247, v247\n\t" \
  "v_exp_f32 v248, v248\n\t" \
  "v_exp_f32 v249, v249\n\t" \
  "v_smfmac_f32_32x32x32_bf16 %[o], %[v14], v[240:247], %[idx]\n\t" \
  "v_exp_f32 v250, v250\n\t" \
  "v_exp_f32 v251, v251\n\t" \
  "v_exp_f32 v252, v252\n\t" \
  "v_exp_f32 v253, v253\n\t" \
  "v_exp_f32 v254, v254\n\t" \
  "v_exp_f32 v255, v255\n\t" \
  "s_nop 1\n\t" \
  "v_smfmac_f32_32x32x32_bf16 %[o], %[v15], v[248:255], %[idx]\n\t" \
  "s_nop 11\n\t"


DEV void attn_publish(char* sm, int t, int w, int l, const f32x16& o, float last) {
  char* pp = sm + (t * 8 + w) * PART;
  *(LAS f32x4*)(pp + l * 16) = f32x4{o[0], o[1], o[2], o[3]};
  *(LAS f32x4*)(pp + 1024 + l * 16) = f32x4{o[4], o[5], o[6], o[7]};
  *(LAS float*)(pp + 2048 + l * 4) = last;
  asm volatile("s_waitcnt lgkmcnt(0)" ::: "memory");
  if (l == 0) { unsigned a = (unsigned)(uintptr_t)(LAS char*)(sm + B_FL + t * 4); asm volatile("ds_add_u32 %0, %1" :: "v"(a), "v"(1u) : "memory"); }
}

DEV void attn_fast(char* sm, const char* __restrict__ ws, int w, int l, float shift, const u32x4 (&kf)[8], const u32x4 (&vf)[16], f32x4 (&acc)[4]) {
  const char* wop = ws + WS_WO + (size_t)w * 8192 + l * 16;
  u32x4 wf[8];
#pragma unroll
  for (int i = 0; i < 8; i++) wf[i] = *(const u32x4*)(wop + i * 1024);
  f32x16 cin;
#pragma unroll
  for (int i = 0; i < 16; i++) cin[i] = -shift;
  u32x4 q = lds128(sm + B_OF + l * 16), qn = lds128(sm + B_OF + 1024 + l * 16);
  f32x16 sa = mfma32(kf[0], q, cin), sb;
  u32x2 z = {0u, 0u}; unsigned idx = 0xDDDDDDDDu;
  asm volatile("s_nop 15" : "+{v[224:239]}"(sa), "+v"(z), "+v"(idx), "+v"(cin));
#pragma unroll 1
  for (int t = 0; t < 8; t++) {
    f32x16 o;
    asm volatile(ATTN_CORE_1 : [o] "=&v"(o), "+{v[224:239]}"(sa), "=&{v[240:255]}"(sb)
      : [k1] "v"(kf[1]), [k2] "v"(kf[2]), [k3] "v"(kf[3]), [k4] "v"(kf[4]), [v0] "v"(vf[0]), [v1] "v"(vf[1]), [v2] "v"(vf[2]), [v3] "v"(vf[3]),
        [v4] "v"(vf[4]), [v5] "v"(vf[5]), [v6] "v"(vf[6]), [q] "v"(q), [cin] "v"(cin), [idx] "v"(idx), [z] "v"(z));
    asm volatile(ATTN_CORE_2 : [o] "+v"(o), "+{v[224:239]}"(sa), "+{v[240:255]}"(sb)
      : [k5] "v"(kf[5]), [k6] "v"(kf[6]), [k7] "v"(kf[7]), [k0] "v"(kf[0]), [v7] "v"(vf[7]), [v8] "v"(vf[8]), [v9] "v"(vf[9]), [v10] "v"(vf[10]),
        [v11] "v"(vf[11]), [v12] "v"(vf[12]), [v13] "v"(vf[13]), [v14] "v"(vf[14]), [v15] "v"(vf[15]), [q] "v"(q), [qn] "v"(qn), [cin] "v"(cin), [idx] "v"(idx));
    q = qn;
    qn = lds128(sm + B_OF + (t < 6 ? t + 2 : 7) * 1024 + l * 16);
    attn_publish(sm, t, w, l, o, o[8]);
    if (w == t - 1) attn_reduce<false>(sm, t - 1, l);
    if (t >= 2) {
      attn_oproj(sm, t - 2, l, wf, acc);
#pragma unroll
      for (int i = 0; i < 8; i++) wf[i] = *(const u32x4*)(wop + (size_t)(t - 1) * 65536 + i * 1024);
    }
  }
  u32x4 wl[8];
#pragma unroll
  for (int i = 0; i < 8; i++) wl[i] = *(const u32x4*)(wop + (size_t)7 * 65536 + i * 1024);
  if (w == 7) attn_reduce<false>(sm, 7, l);
  attn_oproj(sm, 6, l, wf, acc);
  attn_oproj(sm, 7, l, wl, acc);
}

DEV void attn_safe(char* sm, const char* __restrict__ ws, int w, int l, const u32x4 (&kf)[8], const u32x4 (&vf)[16], f32x4 (&acc)[4]) {
  const char* wop = ws + WS_WO + (size_t)w * 8192 + l * 16;
#pragma unroll 1
  for (int t = 0; t < 8; t++) {
    u32x4 q = lds128(sm + B_OF + t * 1024 + l * 16);
    const f32x16 z = {};
    float m = -3.0e38f;
#pragma unroll
    for (int T = 0; T < 8; T++) {
      f32x16 s = mfma32(kf[T], q, z);
#pragma unroll
      for (int i = 0; i < 16; i++) m = fmaxf(m, s[i]);
      __builtin_amdgcn_sched_barrier(0);
    }
    m = max32(m);
    asm volatile("" : "+v"(q));
    f32x16 o = {};
#pragma unroll
    for (int T = 0; T < 8; T++) {
      f32x16 s = mfma32(kf[T], q, z);
      f32x8 p0, p1;
#pragma unroll
      for (int i = 0; i < 8; i++) { p0[i] = __builtin_amdgcn_exp2f(s[i] - m); p1[i] = __builtin_amdgcn_exp2f(s[8 + i] - m); }
      o = __builtin_amdgcn_smfmac_f32_32x32x32_bf16(__builtin_bit_cast(bf16x8, vf[2 * T]), __builtin_bit_cast(bf16x16, p0), o, 0xDDDDDDDD, 0, 0);
      o = __builtin_amdgcn_smfmac_f32_32x32x32_bf16(__builtin_bit_cast(bf16x8, vf[2 * T + 1]), __builtin_bit_cast(bf16x16, p1), o, 0xDDDDDDDD, 0, 0);
      __builtin_amdgcn_sched_barrier(0);
    }
    attn_publish(sm, t, w, l, o, l >= 32 ? m : o[8]);
  }
  attn_reduce<true>(sm, w, l);
#pragma unroll 1
  for (int t = 0; t < 8; t++) {
    u32x4 wf[8];
#pragma unroll
    for (int i = 0; i < 8; i++) wf[i] = *(const u32x4*)(wop + (size_t)t * 65536 + i * 1024);
    attn_oproj(sm, t, l, wf, acc);
  }
}

__global__ void __launch_bounds__(512, 2) k_attn(const u16* __restrict__ xres, char* __restrict__ ws) {
  __shared__ __attribute__((aligned(16))) char sm[B_LDS];
  const int tid = threadIdx.x, l = tid & 63, w = __builtin_amdgcn_readfirstlane(tid >> 6);
  const int b = blockIdx.x, rg = (b & 7) * 32 + (b >> 3);
  if (tid < 16) *(LAS unsigned*)(sm + B_FL + tid * 4) = 0u;
  u32x4 kf[8], vf[16];
  const char* kp = ws + WS_K + w * 8192 + (l & 31) * 32 + (l >> 5) * 16;
  const u32x4 qf = *(const u32x4*)(ws + WS_QF + (size_t)rg * 8192 + tid * 16);
  const float* knp = (const float*)(ws + WS_KN);
  float kn = fmaxf(knp[l], knp[64 + l]);
  float qn = ((const float*)(ws + WS_QN))[(rg >> 2) * 16 + (l & 15)];
#pragma unroll
  for (int T = 0; T < 8; T++) kf[T] = *(const u32x4*)(kp + T * 1024);
  if ((l & 16) == 0) {
    const char* vp = ws + WS_V + w * 8192 + (l >> 5) * 256 + (l & 15) * 16;
#pragma unroll
    for (int i = 0; i < 16; i++) vf[i] = *(const u32x4*)(vp + i * 512);
  } else {
#pragma unroll
    for (int i = 0; i < 16; i++) vf[i] = u32x4{0x3F803F80u, 0x3F803F80u, 0x3F803F80u, 0x3F803F80u};
  }
  const int ri = l & 7, c0 = 64 * w + 16 * (l >> 4) + 8 * ((l >> 3) & 1);
  const u32x4 xr = *(const u32x4*)(xres + (size_t)(rg * 8 + ri) * D + c0);
  *(LAS u32x4*)(sm + B_OF + tid * 16) = qf;
  const float bound = __builtin_sqrtf(wave_max(kn) * wave_max(qn)) * 1.02f;
  asm volatile("s_waitcnt lgkmcnt(0)\n\ts_barrier" ::: "memory");
  f32x4 acc[4] = {};
  if (__builtin_amdgcn_readfirstlane(bound <= 50.0f ? 1 : 0)) attn_fast(sm, ws, w, l, bound, kf, vf, acc);
  else attn_safe(sm, ws, w, l, kf, vf, acc);
  float v[8];
  const bool hi = (l & 8) != 0;
#pragma unroll
  for (int e = 0; e < 4; e++) { v[e] = hi ? acc[2][e] : acc[0][e]; v[4 + e] = hi ? acc[3][e] : acc[1][e]; }
  u32x4 x1;
#pragma unroll
  for (int e = 0; e < 4; e++) x1[e] = pack2(v[2 * e] + bflo(xr[e]), v[2 * e + 1] + bfhi(xr[e]));
  *(u32x4*)(ws + WS_X1 + ((size_t)(rg * 8 + ri) * D + c0) * 2) = x1;
  float sum = 0.f, sq = 0.f;
#pragma unroll
  for (int e = 0; e < 4; e++) { v[2 * e] = bflo(x1[e]); v[2 * e + 1] = bfhi(x1[e]); }
#pragma unroll
  for (int e = 0; e < 8; e++) { sum += v[e]; sq = fmaf(v[e], v[e], sq); }
  sum += dppf<0x128>(sum); sq += dppf<0x128>(sq);
  sum = add32(add16(sum)); sq = add32(add16(sq));
  if (l < 8) *(LAS f32x2*)(sm + B_ST + (ri * 8 + w) * 8) = f32x2{sum, sq};
  asm volatile("s_waitcnt lgkmcnt(0)\n\ts_barrier" ::: "memory");
  sum = 0.f; sq = 0.f;
#pragma unroll
  for (int i = 0; i < 4; i++) { f32x4 t4 = *(const LAS f32x4*)(sm + B_ST + ri * 64 + i * 16); sum += t4[0] + t4[2]; sq += t4[1] + t4[3]; }
  float mean, rinv; ln_scalars(sum, sq, mean, rinv);
  const float nb = -mean * rinv;
  u32x4 xn;
#pragma unroll
  for (int e = 0; e < 4; e++) xn[e] = pack2(fmaf(v[2 * e], rinv, nb), fmaf(v[2 * e + 1], rinv, nb));
  *(u32x4*)(ws + WS_XN1 + (size_t)((rg >> 2) * 32 + 4 * w + (l >> 4)) * 1024 + ((l >> 3) & 1) * 512 + ((rg & 3) * 8 + ri) * 16) = xn;
}

__global__ void __launch_bounds__(512, 2) k_gu(char* __restrict__ ws) {
  __shared__ __attribute__((aligned(16))) char sm[131072];
  const int tid = threadIdx.x, l = tid & 63, w = __builtin_amdgcn_readfirstlane(tid >> 6);
  const int b = blockIdx.x, xcd = b & 7, i = b >> 3;
  const int rb = (xcd >> 2) * 8 + (i >> 2), cb = (xcd & 3) * 4 + (i & 3);
  if (w == 7) {
    const unsigned lds0 = (unsigned)(uintptr_t)(LAS char*)sm;
    const char* xp = ws + WS_XN1 + (size_t)rb * 131072 + l * 16;
#pragma unroll 1
    for (int ch = 0; ch < 8 + 2; ch++) {
      if (ch < 8) {
#pragma unroll
        for (int u = 0; u < 16; u++) glds16(xp + (u & 3) * 32768 + (ch * 4 + (u >> 2)) * 1024, lds0 + (ch * 16 + u) * 1024);
      }
      if (ch >= 2) {
        if (ch < 8) asm volatile("s_waitcnt vmcnt(32)" ::: "memory");
        else if (ch == 8) asm volatile("s_waitcnt vmcnt(16)" ::: "memory");
        else asm volatile("s_waitcnt vmcnt(0)" ::: "memory");
        __builtin_amdgcn_s_barrier();
      }
    }
  } else {
    const int fb = cb * 7 + w;
    const char* wp = ws + WS_WGU + (size_t)fb * 32768 + l * 16;
    u32x4 wf[32];
#pragma unroll
    for (int ks = 0; ks < 32; ks++) wf[ks] = *(const u32x4*)(wp + ks * 1024);
    f32x16 acc[4] = {};
#pragma unroll
    for (int ch = 0; ch < 8; ch++) {
      asm volatile("s_barrier" ::: "memory");
#pragma unroll
      for (int k4 = 0; k4 < 4; k4++)
#pragma unroll
        for (int tt = 0; tt < 4; tt++) {
          u32x4 xf = lds128(sm + (ch * 16 + k4 * 4 + tt) * 1024 + l * 16);
          acc[tt] = mfma32(wf[ch * 4 + k4], xf, acc[tt]);
        }
    }
#pragma unroll
    for (int tt = 0; tt < 4; tt++) {
      u32x4 o;
#pragma unroll
      for (int e = 0; e < 4; e++) o[e] = pack2(acc[tt][2 * e] * acc[tt][8 + 2 * e], acc[tt][2 * e + 1] * acc[tt][9 + 2 * e]);
      *(u32x4*)(ws + WS_H + (size_t)((rb * 4 + tt) * 112 + fb) * 1024 + l * 16) = o;
    }
  }
}

__global__ void __launch_bounds__(256, 1) k_down(char* __restrict__ ws, u16* __restrict__ out) {
  __shared__ __attribute__((aligned(16))) char sm[65536];
  const int tid = threadIdx.x, l = tid & 63, w = __builtin_amdgcn_readfirstlane(tid >> 6);
  const int b = blockIdx.x, rb = (b & 7) * 4 + (b >> 6), cb = (b >> 3) & 7;
  const char* hp = ws + WS_H + (size_t)(rb * 2 * 112 + w * 28) * 1024 + l * 16;
  const char* wp = ws + WS_WD + (size_t)(cb * 2 * 112 + w * 28) * 1024 + l * 16;
  constexpr int WIN = 14;
  u32x4 hf[WIN][2], wf[WIN][2];
#pragma unroll
  for (int s = 0; s < WIN; s++) {
    wf[s][0] = *(const u32x4*)(wp + s * 1024); hf[s][0] = *(const u32x4*)(hp + s * 1024);
    wf[s][1] = *(const u32x4*)(wp + 114688 + s * 1024); hf[s][1] = *(const u32x4*)(hp + 114688 + s * 1024);
  }
  __builtin_amdgcn_sched_barrier(0);
  const int tok = rb * 64 + (w >> 1) * 32 + (l & 31), col = cb * 64 + (w & 1) * 32 + (l >> 5) * 16;
  const u32x4* xp = (const u32x4*)(ws + WS_X1 + ((size_t)tok * D + col) * 2);
  f32x16 acc[4] = {};
#pragma unroll
  for (int s = 0; s < 28; s++) {
    const int u = s % WIN;
    acc[0] = mfma32(wf[u][0], hf[u][0], acc[0]);
    acc[1] = mfma32(wf[u][1], hf[u][0], acc[1]);
    acc[2] = mfma32(wf[u][0], hf[u][1], acc[2]);
    acc[3] = mfma32(wf[u][1], hf[u][1], acc[3]);
    if (s + WIN < 28) {
      wf[u][0] = *(const u32x4*)(wp + (s + WIN) * 1024); hf[u][0] = *(const u32x4*)(hp + (s + WIN) * 1024);
      wf[u][1] = *(const u32x4*)(wp + 114688 + (s + WIN) * 1024); hf[u][1] = *(const u32x4*)(hp + 114688 + (s + WIN) * 1024);
    }
    __builtin_amdgcn_sched_barrier(0);
  }
  const u32x4 xr0 = xp[0], xr1 = xp[1];
#pragma unroll
  for (int tl = 0; tl < 4; tl++)
#pragma unroll
    for (int q = 0; q < 4; q++) *(LAS f32x4*)(sm + (tl * 4 + w) * 4096 + q * 1024 + l * 16) = f32x4{acc[tl][4 * q], acc[tl][4 * q + 1], acc[tl][4 * q + 2], acc[tl][4 * q + 3]};
  __syncthreads();
  f32x4 r[4];
#pragma unroll
  for (int q = 0; q < 4; q++) {
    r[q] = *(const LAS f32x4*)(sm + (w * 4 + 0) * 4096 + q * 1024 + l * 16);
#pragma unroll
    for (int ww = 1; ww < 4; ww++) r[q] += *(const LAS f32x4*)(sm + (w * 4 + ww) * 4096 + q * 1024 + l * 16);
  }
  u32x4 o0 = {pack2(r[0][0] + bflo(xr0[0]), r[0][1] + bfhi(xr0[0])), pack2(r[0][2] + bflo(xr0[1]), r[0][3] + bfhi(xr0[1])),
              pack2(r[1][0] + bflo(xr0[2]), r[1][1] + bfhi(xr0[2])), pack2(r[1][2] + bflo(xr0[3]), r[1][3] + bfhi(xr0[3]))};
  u32x4 o1 = {pack2(r[2][0] + bflo(xr1[0]), r[2][1] + bfhi(xr1[0])), pack2(r[2][2] + bflo(xr1[1]), r[2][3] + bfhi(xr1[1])),
              pack2(r[3][0] + bflo(xr1[2]), r[3][1] + bfhi(xr1[2])), pack2(r[3][2] + bflo(xr1[3]), r[3][3] + bfhi(xr1[3]))};
  u32x4* op = (u32x4*)(out + (size_t)tok * D + col);
  op[0] = o0; op[1] = o1;
}

extern "C" void kernel_launch(void* const* d_in, const int* in_sizes, int n_in,
                              void* d_out, int out_size, void* d_ws, size_t ws_size,
                              hipStream_t stream) {
  const u16* x = (const u16*)d_in[0];
  const u16* wq = (const u16*)d_in[1];
  const u16* wk = (const u16*)d_in[2];
  const u16* wv = (const u16*)d_in[3];
  const u16* wo = (const u16*)d_in[4];
  const u16* wg = (const u16*)d_in[6];
  const u16* wu = (const u16*)d_in[7];
  const u16* wd = (const u16*)d_in[8];
  char* ws = (char*)d_ws;
  k_prep<<<5888 / 4, 256, 0, stream>>>(wo, wg, wu, wd, ws);
  for (int layer = 0; layer < 2; layer++) {
    const u16* xin = layer == 0 ? x : (const u16*)(ws + WS_X2);
    u16* xout = layer == 0 ? (u16*)(ws + WS_X2) : (u16*)d_out;
    k_qkv<<<256, 512, 0, stream>>>(xin, wq, wk, wv, ws);
    k_attn<<<256, 512, 0, stream>>>(xin, ws);
    k_gu<<<256, 512, 0, stream>>>(ws);
    k_down<<<256, 256, 0, stream>>>(ws, xout);
  }
}
```

```cpp
#include <hip/hip_runtime.h>
#include <hip/hip_bf16.h>
#include <stdint.h>

typedef unsigned short u16;
typedef __attribute__((ext_vector_type(2))) __bf16 bf16x2;
typedef __attribute__((ext_vector_type(8))) __bf16 bf16x8;
typedef __attribute__((ext_vector_type(16))) __bf16 bf16x16;
typedef __attribute__((ext_vector_type(2))) float f32x2;
typedef __attribute__((ext_vector_type(4))) float f32x4;
typedef __attribute__((ext_vector_type(8))) float f32x8;
typedef __attribute__((ext_vector_type(16))) float f32x16;
typedef __attribute__((ext_vector_type(2))) unsigned u32x2;
typedef __attribute__((ext_vector_type(4))) unsigned u32x4;
typedef __attribute__((ext_vector_type(8))) unsigned u32x8;
typedef __attribute__((ext_vector_type(16))) unsigned u32x16;
typedef __attribute__((ext_vector_type(4))) short s16x4;
typedef __amdgpu_buffer_rsrc_t rsrc_t;
#define LAS __attribute__((address_space(3)))
#define DEV __device__ __forceinline__
#define SBAR() __builtin_amdgcn_sched_barrier(0)

constexpr int PSLEEP = 22, XSLEEP = 4, WDE = 10, P1 = 3, P2 = 11, P3 = 26, PS1 = 4, PS2 = 10, EW = 9, QD = 9, QPRE = 13, XRE = 18, JW = 0, SW = 1, PW = 0;
constexpr float SHIFT = 44.0f;
constexpr float FAST_LIMIT = SHIFT / 1.02f;
constexpr int S = 2048, D = 512, F = 1792;
constexpr float QSCALE = 0.25f * 1.4426950408889634f;

constexpr unsigned WS_FLAG = 0;
constexpr unsigned FL_B = 0, FL_G = 2 * 65536;
constexpr unsigned WS_CS  = 262144;
constexpr unsigned WS_WO  = 524288;
constexpr unsigned WS_WGU = WS_WO + 524288;
constexpr unsigned WS_WD  = WS_WGU + 3670016;
constexpr unsigned WS_WQ  = WS_WD + 1835008;
constexpr unsigned WS_WKV = WS_WQ + 524288;
constexpr unsigned WS_X2  = WS_WKV + 32768;
constexpr unsigned WS_LAY = WS_X2 + 2097152;
constexpr unsigned L_QF  = 0;
constexpr unsigned L_K   = L_QF + 2097152;
constexpr unsigned L_V   = L_K + 65536;
constexpr unsigned L_KN  = L_V + 65536;
constexpr unsigned L_X1  = L_KN + 16384;
constexpr unsigned L_XN1 = L_X1 + 2097152;
constexpr unsigned L_H   = L_XN1 + 2097152;
constexpr unsigned L_SZ  = L_H + 7340032;
constexpr unsigned WS_END = WS_LAY + 2 * L_SZ;

DEV unsigned pack2(float lo, float hi) { f32x2 v = {lo, hi}; return __builtin_bit_cast(unsigned, __builtin_convertvector(v, bf16x2)); }
DEV float bflo(unsigned u) { return __uint_as_float(u << 16); }
DEV float bfhi(unsigned u) { return __uint_as_float(u & 0xffff0000u); }
DEV float dot2(unsigned a, unsigned b, float c) { return __builtin_amdgcn_fdot2_f32_bf16(__builtin_bit_cast(bf16x2, a), __builtin_bit_cast(bf16x2, b), c, false); }
DEV unsigned umax4(u32x4 v, unsigned m) { return max(max(max(max(m, v[0]), v[1]), v[2]), v[3]); }
DEV bool fresh(unsigned m) { return __all(m != 0xFFFFFFFFu); }
DEV u32x4 nomark(u32x4 v) { return u32x4{min(v[0], 0xFFFFFFFEu), min(v[1], 0xFFFFFFFEu), min(v[2], 0xFFFFFFFEu), min(v[3], 0xFFFFFFFEu)}; }
constexpr int RETRY = 1 << 16;
DEV float rbf(float v) { return bflo(pack2(v, v)); }
DEV f32x16 mfma32(u32x4 a, u32x4 b, f32x16 c) { return __builtin_amdgcn_mfma_f32_32x32x16_bf16(__builtin_bit_cast(bf16x8, a), __builtin_bit_cast(bf16x8, b), c, 0, 0, 0); }
DEV f32x4 mfma16(u32x4 a, u32x4 b, f32x4 c) { return __builtin_amdgcn_mfma_f32_16x16x32_bf16(__builtin_bit_cast(bf16x8, a), __builtin_bit_cast(bf16x8, b), c, 0, 0, 0); }
template <int CTRL> DEV float dppf(float v) { return __int_as_float(__builtin_amdgcn_update_dpp(0, __float_as_int(v), CTRL, 0xF, 0xF, true)); }
DEV void sw32(unsigned& a, unsigned& b) { auto r = __builtin_amdgcn_permlane32_swap(a, b, false, false); unsigned r0 = r[0], r1 = r[1]; a = r0; b = r1; }
DEV void sw16(unsigned& a, unsigned& b) { auto r = __builtin_amdgcn_permlane16_swap(a, b, false, false); unsigned r0 = r[0], r1 = r[1]; a = r0; b = r1; }
DEV float add32(float v) { unsigned a = __float_as_uint(v), b = a; sw32(a, b); return __uint_as_float(a) + __uint_as_float(b); }
DEV float max32(float v) { unsigned a = __float_as_uint(v), b = a; sw32(a, b); return fmaxf(__uint_as_float(a), __uint_as_float(b)); }
DEV float add16(float v) { unsigned a = __float_as_uint(v), b = a; sw16(a, b); return __uint_as_float(a) + __uint_as_float(b); }
template <int CTRL> DEV unsigned dppu(unsigned v) { return (unsigned)__builtin_amdgcn_update_dpp(0, (int)v, CTRL, 0xF, 0xF, true); }
DEV unsigned row_umax(unsigned v) { v = max(v, dppu<0xB1>(v)); v = max(v, dppu<0x4E>(v)); v = max(v, dppu<0x141>(v)); return max(v, dppu<0x140>(v)); }
DEV void glds16(const void* g, unsigned lds_base) {
  unsigned sv; asm volatile("s_mov_b32 %0, m0\n\ts_mov_b32 m0, %2\n\ts_nop 0\n\tglobal_load_lds_dwordx4 %1, off\n\ts_mov_b32 m0, %0" : "=&s"(sv) : "v"(g), "s"(lds_base) : "memory"); }
DEV u32x4 lds128(const char* p) { return *(const LAS u32x4*)(p); }
DEV u32x2 ldstr(const char* p) { return __builtin_bit_cast(u32x2, __builtin_amdgcn_ds_read_tr16_b64_v4i16((LAS s16x4*)(p))); }
DEV int lane_id() { int l; asm volatile("v_mbcnt_lo_u32_b32 %0, -1, 0\n\tv_mbcnt_hi_u32_b32 %0, -1, %0" : "=v"(l)); __builtin_assume(l >= 0 && l < 64); return l; }
DEV rsrc_t mkrs(const void* p, unsigned bytes) { return __builtin_amdgcn_make_buffer_rsrc((void*)p, 0, bytes, 0x00020000); }
DEV u32x4 bld(rsrc_t r, unsigned vo, unsigned so) { return __builtin_amdgcn_raw_buffer_load_b128(r, vo, so, 0); }
DEV u32x4 bld1(rsrc_t r, unsigned vo, unsigned so) { return __builtin_amdgcn_raw_buffer_load_b128(r, vo, so, 16); }
DEV void bst1(u32x4 v, rsrc_t r, unsigned vo, unsigned so) { __builtin_amdgcn_raw_buffer_store_b128(v, r, vo + so, 0, 16); }
DEV void ln_scalars(float sum, float sq, float& mean_b, float& rinv) {
  float mean = sum * (1.0f / D);
  float var = fmaxf(sq * (1.0f / D) - mean * mean, 0.0f);
  mean_b = rbf(mean);
  float sd = rbf(__builtin_amdgcn_sqrtf(rbf(rbf(var) + 1e-5f)));
  rinv = __builtin_amdgcn_rcpf(sd);
}
DEV void flag_add(rsrc_t r, unsigned vo) { asm volatile("buffer_atomic_add %0, %1, %2, 0 offen" :: "v"(1u), "v"(vo), "s"(r) : "memory"); }
DEV void flag_wait(rsrc_t r, unsigned vo, unsigned target) {
#pragma unroll 1
  for (int sp = 0; sp < (1 << 16); sp++) {
    const unsigned v = __builtin_amdgcn_raw_buffer_load_b32(r, vo, 0, 16);
    if (__all(v >= target)) break;
    __builtin_amdgcn_s_sleep(1);
    asm volatile("" ::: "memory");
  }
  asm volatile("" ::: "memory");
}

DEV void btouch(rsrc_t r, unsigned vo, unsigned so, unsigned lds_base) {
  unsigned sv; asm volatile("s_mov_b32 %0, m0\n\ts_mov_b32 m0, %4\n\ts_nop 0\n\tbuffer_load_dword %1, %2, %3 offen lds\n\ts_mov_b32 m0, %0" : "=&s"(sv) : "v"(vo), "s"(r), "s"(so), "s"(lds_base) : "memory"); }
DEV void lds_wait_ge(const char* p, unsigned v) {
  unsigned a = (unsigned)(uintptr_t)(const LAS char*)p, tv, ta, ts, cnt;
  asm volatile("s_mov_b32 %2, 0x200000\n\tv_mov_b32 %3, %4\n"
               "1%=:\n\tds_read_b32 %0, %3\n\ts_waitcnt lgkmcnt(0)\n\tv_readfirstlane_b32 %1, %0\n\ts_cmp_ge_u32 %1, %5\n\ts_cbranch_scc1 2%=f\n\t"
               "s_sleep 1\n\ts_sub_u32 %2, %2, 1\n\ts_cbranch_scc0 1%=b\n"
               "2%=:" : "=&v"(tv), "=&s"(ts), "=&s"(cnt), "=&v"(ta) : "s"(a), "i"(v) : "memory", "scc");
}
DEV void lds_arrive(char* p) {
  unsigned a = (unsigned)(uintptr_t)(LAS char*)p, ta, one;
  asm volatile("v_mov_b32 %0, %2\n\tv_mov_b32 %1, 1\n\ts_mov_b64 exec, 1\n\tds_add_u32 %0, %1\n\ts_mov_b64 exec, -1" : "=&v"(ta), "=&v"(one) : "s"(a) : "memory");
}
DEV unsigned lds_peek(const char* p) { unsigned a = (unsigned)(uintptr_t)(const LAS char*)p; asm volatile("" : "+s"(a)); return *(volatile const LAS unsigned*)(uintptr_t)a; }

DEV void code_touch(int piece, int l, unsigned lds_pad) {
  unsigned long long pc; unsigned len;
  asm volatile("s_getpc_b64 %0\nLpc%=:\n\ts_mov_b32 %1, k_main_code_end - Lpc%=" : "=s"(pc), "=s"(len));
  btouch(mkrs((const void*)pc, len), l * 64, piece * 4096, lds_pad);
}

__global__ void __launch_bounds__(832, 4) k_pre(const u16* x, const u16* wq, const u16* wk, const u16* wv, const u16* wo, const u16* wg, const u16* wu, const u16* wd, char* ws) {
  __shared__ __attribute__((aligned(16))) char sm[147456 + 256 + 16 + 512];
  char* XF = sm; char* W = sm + 65536; char* WKV = sm + 131072; char* ST = sm + 147456 + 272;
  const u32x4 ones = {0x3F803F80u, 0x3F803F80u, 0x3F803F80u, 0x3F803F80u};
  const int tid = threadIdx.x, l = tid & 63, w = __builtin_amdgcn_readfirstlane(tid >> 6);
  const int b = blockIdx.x, rb = (b & 7) * 4 + (b >> 6), cb = (b >> 3) & 7;
  const unsigned lds0 = (unsigned)(uintptr_t)(LAS char*)sm;
  if (w >= 8) {
    asm volatile("s_barrier" ::: "memory");
    const int task = (w - 8) * 256 + b, c = l & 7;
    if (w == 12) {
      if (l < 48) *(u32x4*)(ws + WS_FLAG + ((l >> 4) * 256 + b) * 256 + (l & 15) * 16) = u32x4{0u, 0u, 0u, 0u};
      if ((b >> 3) >= 8 && (b >> 3) < 20) code_touch((b >> 3) - 8, l, lds0 + 147456);
    }
    if (task < 804) {
    u32x4 pv[8]; unsigned pdst = 0, dA = 16, dA2 = 32, dB = 0;
    const u16* src; int sA, sA2, sB; const int kq = l >> 3;
    if (task < 448) {
      const int R = task >> 4, ks = task & 15, sel = (l >> 3) & 1, kg = l >> 4;
      src = (sel ? wu : wg) + (size_t)(32 * ks + 8 * kg) * F + 64 * R + 8 * c; sA = F; sA2 = 2 * F; sB = 4 * F;
      pdst = WS_WGU + (8 * R * 16 + ks) * 1024 + (kg * 16 + 4 * (c >> 1) + 2 * sel + (c & 1)) * 16; dA = 16384; dA2 = 32768; dB = 65536;
    } else if (task < 672) {
      const int g = task - 448, c8 = g & 7, R = g >> 3;
      src = wd + (size_t)(64 * R + kq) * D + 64 * c8 + 8 * c; sA = 8 * D; sA2 = 16 * D; sB = 32 * D;
      pdst = WS_WD + ((c8 * 56 + 2 * R + (kq >> 2)) * 4 + 2 * (c & 1)) * 1024 + ((kq & 3) * 16 + 4 * (c >> 1)) * 16; dB = 1024;
    } else if (task < 736) {
      const int g = task - 672, c8 = g & 7, t = g >> 3;
      src = wo + (size_t)(64 * t + 8 * kq) * D + 64 * c8 + 8 * c; sA = D; sA2 = 4 * D; sB = 2 * D;
      pdst = WS_WO + ((t * 8 + c8) * 8 + (kq >> 2)) * 1024 + ((kq & 3) * 16 + c) * 16; dA = 2048; dA2 = 4096; dB = 128;
    } else if (task < 800) {
      const int g = task - 736, c8 = g & 7, R = g >> 3;
      src = wq + (size_t)(64 * R + 8 * kq) * D + 64 * c8 + 8 * c; sA = D; sA2 = 2 * D; sB = 4 * D;
      pdst = WS_WQ + ((4 * c8 + (c >> 1)) * 16 + 2 * R + (kq >> 2)) * 1024 + ((kq & 3) * 16 + 8 * (c & 1)) * 16; dB = 64;
    } else {
      const int g = task - 800, R = g & 1, k2 = l >> 1;
      src = ((g >> 1) ? wv : wk) + (size_t)(256 * R + 8 * k2) * 16 + 8 * (l & 1); sA = 16; sA2 = 32; sB = 64;
      pdst = WS_WKV + ((g >> 1) * 16 + 8 * R + (k2 >> 2)) * 1024 + ((k2 & 3) * 16 + 8 * (l & 1)) * 16; dB = 64;
    }
    __builtin_amdgcn_s_sleep(PSLEEP);
#pragma unroll
    for (int j = 0; j < 8; j++) pv[j] = *(const u32x4*)(src + (j & 1) * sA + ((j >> 1) & 1) * sA2 + (j >> 2) * sB);
#pragma unroll
    for (int i = 0; i < 8; i++) {
      u32x4 o;
#pragma unroll
      for (int e = 0; e < 4; e++) o[e] = __builtin_amdgcn_perm(pv[2 * e + 1][i >> 1], pv[2 * e][i >> 1], (i & 1) ? 0x07060302u : 0x05040100u);
      if (task < 448 || (task >= 672 && task < 736))     __builtin_amdgcn_raw_buffer_store_b128(o, mkrs(ws, WS_END), pdst + (i & 1) * dA + ((i >> 1) & 1) * dA2 + (i >> 2) * dB, 0, 16);
      else *(u32x4*)(ws + pdst + (i & 1) * dA + ((i >> 1) & 1) * dA2 + (i >> 2) * dB) = o;
    }
    }
  if (w == 12) {
    __builtin_amdgcn_s_setprio(3);
    char* lay = ws + WS_LAY;
    const int kg = l >> 4, q = (l & 15) >> 2, p = l & 3;
    const int t16 = cb & 3, T = rb * 2 + (t16 >> 1);
    const bool isV = (cb & 4) != 0;
    const char* wp = WKV + (8 * kg + q) * 32 + 8 * p;
    const char* xq = XF + t16 * 16384 + kg * 256;
    f32x4 ak = {}, av = {};
#pragma unroll
    for (int h = 0; h < 2; h++) {
      lds_wait_ge(sm + 147456 + 256 + 4 * h, 8u);
      u32x4 wf[8], xf[8];
#pragma unroll
      for (int u = 0; u < 8; u++) {
        const int ks = h * 8 + u;
        const u32x2 w0 = ldstr(wp + ks * 1024), w1 = ldstr(wp + ks * 1024 + 128);
        wf[u] = u32x4{w0[0], w0[1], w1[0], w1[1]};
        xf[u] = lds128(xq + ks * 1024 + (((l & 15) ^ (4 * (ks & 1) + kg)) * 16));
      }
      SBAR();
      if (isV) {
#pragma unroll
        for (int u = 0; u < 8; u++) { av = mfma16(xf[u], wf[u], av); ak = mfma16(ones, wf[u], ak); }
      } else {
#pragma unroll
        for (int u = 0; u < 8; u++) { av = mfma16(wf[u], xf[u], av); ak = mfma16(wf[u], ones, ak); }
      }
    }
    f32x4 aj;
    if (isV) {
      aj = (av + *(const LAS f32x4*)(ST + (t16 * 16 + kg * 4) * 4) * ak) * *(const LAS f32x4*)(ST + 256 + (t16 * 16 + kg * 4) * 4);
      if (rb == 0 && t16 == 0 && l < 16) *(float*)(ws + WS_CS + 2112 + l * 4) = ak[0];
    } else {
      aj = (av + *(const LAS float*)(ST + (t16 * 16 + (l & 15)) * 4) * ak) * *(const LAS float*)(ST + 256 + (t16 * 16 + (l & 15)) * 4);
      if (rb == 0 && t16 == 0 && (l & 15) == 0) *(f32x4*)(ws + WS_CS + 2048 + kg * 16) = ak;
    }
    unsigned e0 = pack2(aj[0], aj[1]), e1 = pack2(aj[2], aj[3]), f0 = e0, f1 = e1;
    sw16(e0, f0); sw16(e1, f1);
    if ((l & 16) == 0) *(u32x4*)(lay + (isV ? L_V : L_K) + T * 1024 + (t16 & 1) * 256 + ((l >> 5) * 32 + (l & 15)) * 16) = u32x4{e0, e1, f0, f1};
    if (!isV) {
      float n = aj[0] * aj[0] + aj[1] * aj[1] + aj[2] * aj[2] + aj[3] * aj[3];
      const unsigned nk = row_umax(__float_as_uint(add32(add16(n))));
      if (l < 32) ((unsigned*)(lay + L_KN))[(T * 2 + (t16 & 1)) * 32 + l] = nk;
    }
  }
    return;
  }
  const int row = 8 * w + (l >> 3), c = l & 7;
  const u16* xp = x + (size_t)(rb * 64 + row) * D + c * 8;
  u32x4 xv[8];
#define XLD(i) asm volatile("global_load_dwordx4 %0, %1, off offset:%2" : "=v"(xv[i]) : "v"(xp), "i"((i) * 128) : "memory")
  const u16* wsrc = wq + (size_t)(w * 8 + (l >> 3)) * D + cb * 64 + (c ^ (((l >> 4) & 1) * 4 + (w & 1) * 2)) * 8;
  const u16* ksrc = ((cb & 4) ? wv : wk) + w * 512 + l * 8;
  XLD(0); XLD(1); XLD(2); XLD(3);
#pragma unroll
  for (int i = 0; i < 4; i++) glds16(wsrc + (size_t)i * 64 * D, lds0 + 65536 + (i * 8 + w) * 1024);
  glds16(ksrc, lds0 + 131072 + w * 1024);
  if (tid < 2) *(volatile LAS unsigned*)(sm + 147456 + 256 + tid * 4) = 0u;
  asm volatile("s_waitcnt lgkmcnt(0)\n\ts_barrier" ::: "memory");
  XLD(4); XLD(5); XLD(6); XLD(7);
#pragma unroll
  for (int i = 4; i < 8; i++) glds16(wsrc + (size_t)i * 64 * D, lds0 + 65536 + (i * 8 + w) * 1024);
  glds16(ksrc + 4096, lds0 + 131072 + (8 + w) * 1024);
  char* dst = XF + (row >> 4) * 16384 + c * 256 + (((row & 15) ^ c) * 16);
  float sum = 0.f, sq = 0.f, sum2 = 0.f, sq2 = 0.f;
#define XSUM(i) _Pragma("unroll") for (int e = 0; e < 4; e += 2) { sum = dot2(xv[i][e], 0x3F803F80u, sum); sq = dot2(xv[i][e], xv[i][e], sq); sum2 = dot2(xv[i][e + 1], 0x3F803F80u, sum2); sq2 = dot2(xv[i][e + 1], xv[i][e + 1], sq2); }
  asm volatile("s_waitcnt vmcnt(14)" : "+v"(xv[0]), "+v"(xv[1]), "+v"(xv[2]), "+v"(xv[3]) :: "memory");
#pragma unroll
  for (int i = 0; i < 4; i++) *(LAS u32x4*)(dst + i * 2048) = xv[i];
  SBAR();
  XSUM(0); XSUM(1); XSUM(2); XSUM(3);
  asm volatile("s_waitcnt vmcnt(9) lgkmcnt(0)" : "+v"(sum), "+v"(sq), "+v"(sum2), "+v"(sq2) :: "memory");
  lds_arrive(sm + 147456 + 256);
  lds_wait_ge(sm + 147456 + 256, 8u);
  char* lay = ws + WS_LAY;
  const int kg = l >> 4, q = (l & 15) >> 2, p = l & 3;
  {
    const int hq = w & 3, th = w >> 2, T = rb * 2 + th, head = cb * 4 + hq;
    const char* wp = W + (8 * kg + q) * 128 + (((2 * hq + (p >> 1)) ^ ((q >> 1) * 4 + (kg & 1) * 2)) * 16) + 8 * (p & 1);
    const char* xq = XF + th * 32768 + kg * 256;
    f32x4 a0 = {}, a1 = {}, cs = {};
    u32x4 wf[3][2], x0[3][2], x1[3][2];
#define QRD(g) _Pragma("unroll") for (int u = 0; u < 2; u++) { const int ks = (g) * 2 + u; const u32x2 w0 = ldstr(wp + ks * 4096), w1 = ldstr(wp + ks * 4096 + 512); \
      wf[(g) % 3][u] = u32x4{w0[0], w0[1], w1[0], w1[1]}; const int so = ks * 1024 + (((l & 15) ^ (4 * (ks & 1) + kg)) * 16); \
      x0[(g) % 3][u] = lds128(xq + so); x1[(g) % 3][u] = lds128(xq + 16384 + so); }
    QRD(0); QRD(1);
#pragma unroll
    for (int g = 0; g < 8; g++) {
      if (g == 4) {
        asm volatile("s_waitcnt vmcnt(5)" : "+v"(xv[4]), "+v"(xv[5]), "+v"(xv[6]), "+v"(xv[7]) :: "memory");
#pragma unroll
        for (int i = 4; i < 8; i++) *(LAS u32x4*)(dst + i * 2048) = xv[i];
        SBAR();
        XSUM(4); XSUM(5); XSUM(6); XSUM(7);
        sum += sum2; sq += sq2;
        sum += dppf<0xB1>(sum); sq += dppf<0xB1>(sq); sum += dppf<0x4E>(sum); sq += dppf<0x4E>(sq); sum += dppf<0x141>(sum); sq += dppf<0x141>(sq);
        float mean, rinv; ln_scalars(sum, sq, mean, rinv);
        if (c < 2) *(LAS float*)(ST + c * 256 + row * 4) = c ? rinv : -mean;
        asm volatile("s_waitcnt vmcnt(0) lgkmcnt(0)" ::: "memory");
        lds_arrive(sm + 147456 + 260);
        lds_wait_ge(sm + 147456 + 260, 8u);
        QRD(4); QRD(5);
      }
      if ((g & 3) < 2) { QRD(g + 2); }
      SBAR();
#pragma unroll
      for (int u = 0; u < 2; u++) { a0 = mfma16(wf[g % 3][u], x0[g % 3][u], a0); a1 = mfma16(wf[g % 3][u], x1[g % 3][u], a1); cs = mfma16(wf[g % 3][u], ones, cs); }
      SBAR();
    }
    const float nm0 = *(const LAS float*)(ST + (th * 32 + (l & 15)) * 4), nm1 = *(const LAS float*)(ST + (th * 32 + 16 + (l & 15)) * 4);
    const float ri0 = *(const LAS float*)(ST + 256 + (th * 32 + (l & 15)) * 4), ri1 = *(const LAS float*)(ST + 256 + (th * 32 + 16 + (l & 15)) * 4);
    a0 = (a0 + nm0 * cs) * (ri0 * QSCALE); a1 = (a1 + nm1 * cs) * (ri1 * QSCALE);
    if (T == 0 && (l & 15) == 0) *(f32x4*)(ws + WS_CS + head * 64 + kg * 16) = cs;
    unsigned p0 = pack2(a0[0], a0[1]), p1 = pack2(a0[2], a0[3]), q0 = pack2(a1[0], a1[1]), q1 = pack2(a1[2], a1[3]);
    sw16(p0, q0); sw16(p1, q1);
    __builtin_amdgcn_raw_buffer_store_b128(u32x4{p0, p1, q0, q1}, mkrs(ws, WS_END), WS_LAY + L_QF + T * 32768 + (head >> 2) * 1024 + (head & 3) * 128 + ((kg & 1) * 2 + ((l >> 3) & 1)) * 8192 + ((kg >> 1) * 32 + (l & 7)) * 16, 0, 16);
  }
}

constexpr int PART = 2304;
constexpr int PSLOT = 8 * PART;
constexpr int B_WP = 2 * PSLOT;
constexpr int NPF = 14, NRG = 5;
constexpr int B_OF = B_WP + 8 * NPF * 1024;
constexpr int B_ST = B_OF + 8192;
constexpr int B_FL = B_ST + 512;
constexpr int B_LDS = B_FL + 128;
static_assert(8 * PSLOT <= B_OF, "fallback slots");
constexpr int FW_H = B_FL + 68, FW_G = B_FL + 76, FW_S = B_FL + 80;
static_assert(B_WP + 8 * NPF * 1024 <= B_OF && 2 * PSLOT <= B_WP && B_LDS <= 163840, "LDS map");

DEV void bdma16(rsrc_t r, unsigned vo, unsigned so, unsigned lds_base) {
  unsigned sv; asm volatile("s_mov_b32 %0, m0\n\ts_mov_b32 m0, %4\n\ts_nop 0\n\tbuffer_load_dwordx4 %1, %2, %3 offen lds\n\ts_mov_b32 m0, %0" : "=&s"(sv) : "v"(vo), "s"(r), "s"(so), "s"(lds_base) : "memory"); }


DEV void lds_set(char* p, unsigned v) { asm volatile("s_waitcnt lgkmcnt(0)" ::: "memory"); *(volatile LAS unsigned*)p = v; }

DEV void attn_reduce(char* sm, int t, int slot, int l) {
  lds_wait_ge(sm + B_FL + t * 4, 8u);
  const char* pp = sm + slot * PSLOT;
  f32x4 a0 = {}, a1 = {}; float ls = 0.f;
  float mw[8], mm = 0.f;
  {
#pragma unroll
    for (int ww = 0; ww < 8; ww++) mw[ww] = *(const LAS float*)(pp + ww * PART + 2048 + 128 + (l & 31) * 4);
    mm = mw[0];
#pragma unroll
    for (int ww = 1; ww < 8; ww++) mm = fmaxf(mm, mw[ww]);
  }
#pragma unroll
  for (int ww = 0; ww < 8; ww++) {
    f32x4 p0 = *(const LAS f32x4*)(pp + ww * PART + l * 16), p1 = *(const LAS f32x4*)(pp + ww * PART + 1024 + l * 16);
    float lv = *(const LAS float*)(pp + ww * PART + 2048 + (l & 31) * 4);
    { float f = __builtin_amdgcn_exp2f(mw[ww] - mm); p0 *= f; p1 *= f; lv *= f; }
    if (ww == 0) { a0 = p0; a1 = p1; ls = lv; } else { a0 += p0; a1 += p1; ls += lv; }
  }
  const float inv = __builtin_amdgcn_rcpf(ls);
  u32x4 o = {pack2(a0[0] * inv, a0[1] * inv), pack2(a0[2] * inv, a0[3] * inv), pack2(a1[0] * inv, a1[1] * inv), pack2(a1[2] * inv, a1[3] * inv)};
  *(LAS u32x4*)(sm + B_OF + t * 1024 + l * 16) = o;
  lds_set(sm + B_FL + 32 + t * 4, 1u);
}

DEV void attn_oproj(char* sm, int t, int l, const u32x4 (&wf)[8], f32x4 (&acc)[4]) {
  lds_wait_ge(sm + B_FL + 32 + t * 4, 1u);
  const u32x4 of = lds128(sm + B_OF + t * 1024 + (((l >> 3) & 1) * 32 + (l >> 4) * 8 + (l & 7)) * 16);
  const int idx = (l & 8) ? 0xEEEEEEEE : 0x44444444;
#pragma unroll
  for (int nt = 0; nt < 4; nt++) {
    const u32x8 wb = {wf[2 * nt][0], wf[2 * nt][1], wf[2 * nt][2], wf[2 * nt][3], wf[2 * nt + 1][0], wf[2 * nt + 1][1], wf[2 * nt + 1][2], wf[2 * nt + 1][3]};
    acc[nt] = __builtin_amdgcn_smfmac_f32_16x16x64_bf16(__builtin_bit_cast(bf16x8, of), __builtin_bit_cast(bf16x16, wb), acc[nt], idx, 0, 0);
  }
}

#define ATTN_FIRST \
  "v_mbcnt_lo_u32_b32 v225, -1, 0\n\t" \
  "v_mbcnt_hi_u32_b32 v225, -1, v225\n\t" \
  "s_lshl_b32 s77, %[lin], 13\n\t" \
  "s_add_u32 s77, s77, %[lay]\n\t" \
  "s_lshl_b32 s68, %[w], 10\n\t" \
  "s_add_u32 s69, s68, s77\n\t" \
  "v_lshlrev_b32 v224, 4, v225\n\t" \
  "v_and_b32 v230, 15, v225\n\t" \
  "v_lshlrev_b32 v230, 7, v230\n\t" \
  "s_lshl_b32 s66, %[w], 11\n\t" \
  "s_add_u32 s66, s66, %[lay]\n\t" \
  "s_add_u32 s66, s66, 2228224\n\t" \
  "s_lshl_b32 s76, %[w], 13\n\t" \
  "s_add_u32 s76, s76, %[lay]\n\t" \
  "s_add_u32 s78, s76, 2162688\n\t" \
  "s_add_u32 s76, s76, 2097152\n\t" \
  "s_cmp_eq_u32 %[gw], 0\n\t" \
  "s_cbranch_scc1 LGO%=\n\t" \
  "s_cmp_lg_u32 %[w], 0\n\t" \
  "s_cbranch_scc1 LGB%=\n\t" \
  "s_lshr_b32 s68, %[lin], 5\n\t" \
  "s_lshl_b32 s68, s68, 11\n\t" \
  "s_add_u32 s68, s68, 131072\n\t" \
  "v_and_b32 v144, 7, v225\n\t" \
  "v_lshlrev_b32 v144, 8, v144\n\t" \
  "s_mov_b32 s72, 0x10000\n\t" \
  "LGP%=:\n\t" \
  "buffer_load_dword v145, v144, %[rs], s68 offen sc1\n\t" \
  "s_waitcnt vmcnt(0)\n\t" \
  "v_cmp_gt_u32 vcc, 32, v145\n\t" \
  "s_and_b64 vcc, vcc, exec\n\t" \
  "s_cbranch_scc0 LGB%=\n\t" \
  "s_sleep 1\n\t" \
  "s_sub_u32 s72, s72, 1\n\t" \
  "s_cbranch_scc0 LGP%=\n\t" \
  "LGB%=:\n\t" \
  "s_barrier\n\t" \
  "LGO%=:\n\t" \
  "buffer_load_dwordx4 v[176:179], v224, %[rs], s69 offen sc1\n\t" \
  "buffer_load_dword v144, v230, %[rs], s66 offen sc1\n\t" \
  "buffer_load_dwordx4 v[208:211], v224, %[rs], s77 offen sc1\n\t" \
  "buffer_load_dwordx4 v[0:3], v224, %[rs], s76 offen offset:0 sc1\n\t" \
  "buffer_load_dwordx4 v[32:35], v224, %[rs], s78 offen offset:0 sc1\n\t"

#define ATTN_FIRST_CLOBBERS "v224", "v225", "v229", "v230", "s60", "s61", "s62", "s63", "s64", "s65", "s66", "s67", "s68", "s69", "s70", "s71", "s72", "s73", "s74", "s75", "s76", "s77", "s78", "s79", "vcc", "scc", "memory"
#define ATTN_LOOP \
  "s_mov_b32 s70, m0\n\t" \
  "v_mbcnt_lo_u32_b32 v225, -1, 0\n\t" \
  "v_mbcnt_hi_u32_b32 v225, -1, v225\n\t" \
  "s_lshl_b32 s68, %[w], 10\n\t" \
  "v_lshlrev_b32 v224, 4, v225\n\t" \
  "v_lshlrev_b32 v229, 2, v225\n\t" \
  "v_and_b32 v230, 0x7c, v229\n\t" \
  "s_lshl_b32 s60, %[w], 13\n\t" \
  "s_add_u32 s76, s60, %[lay]\n\t" \
  "s_add_u32 s78, s76, 2162688\n\t" \
  "s_add_u32 s76, s76, 2097152\n\t" \
  "s_add_u32 s77, s76, 4096\n\t" \
  "s_add_u32 s79, s78, 4096\n\t" \
  "s_add_u32 s75, %[lds], 151552\n\t" \
  "v_add_u32 v226, s75, v224\n\t" \
  "v_add_u32 v212, s68, v226\n\t" \
  "v_and_b32 v227, 0x70, v224\n\t" \
  "v_and_b32 v228, 8, v225\n\t" \
  "v_lshl_or_b32 v227, v228, 6, v227\n\t" \
  "v_cmp_eq_u32 vcc, 0, v228\n\t" \
  "v_and_b32 v228, 48, v225\n\t" \
  "v_lshl_or_b32 v227, v228, 3, v227\n\t" \
  "v_mov_b32 v229, 0x44444444\n\t" \
  "v_mov_b32 v228, 0xeeeeeeee\n\t" \
  "v_cndmask_b32 v229, v228, v229, vcc\n\t" \
  "v_add_u32 v227, s75, v227\n\t" \
  "v_and_b32 v231, 0x380, v224\n\t" \
  "v_mad_u32_u24 v231, v231, 7, v224\n\t" \
  "s_add_u32 s69, %[lds], 160256\n\t" \
  "v_add_u32 v213, s69, v230\n\t" \
  "v_mov_b32 v230, s69\n\t" \
  "s_lshl_b32 s72, %[w], 1\n\t" \
  "s_cmp_lt_u32 %[w], 4\n\t" \
  "s_cselect_b32 s66, 0, -7\n\t" \
  "s_add_u32 s72, s72, s66\n\t" \
  "s_lshl_b32 s74, s72, 2\n\t" \
  "s_add_u32 s74, s74, s69\n\t" \
  "s_lshl_b32 s68, s72, 10\n\t" \
  "s_add_u32 s75, s75, s68\n\t" \
  "s_and_b32 s73, s72, 1\n\t" \
  "s_mul_i32 s73, s73, 18432\n\t" \
  "s_add_u32 s73, s73, %[lds]\n\t" \
  "s_add_u32 s72, s72, 1\n\t" \
  "s_mul_i32 s69, %[w], 2304\n\t" \
  "s_add_u32 s69, s69, %[lds]\n\t" \
  "v_add_u32 v228, s69, v224\n\t" \
  "s_mov_b32 s59, s69\n\t" \
  "s_lshl_b32 s69, %[w], 3\n\t" \
  "s_add_u32 s69, s69, %[lds]\n\t" \
  "s_add_u32 s69, s69, 159744\n\t" \
  "v_and_b32 v214, 32, v225\n\t" \
  "v_bfe_u32 v215, v225, 4, 1\n\t" \
  "v_lshlrev_b32 v214, 1, v214\n\t" \
  "v_lshl_add_u32 v214, v215, 2, v214\n\t" \
  "v_add_u32 v214, s69, v214\n\t" \
  "v_add_u32 v215, -512, v213\n\t" \
  "v_mov_b32 v146, 0\n\t" \
  "v_and_b32 v223, 15, v225\n\t" \
  "v_cmp_eq_u32 vcc, 0, v223\n\t" \
  "v_mov_b32 v220, 0x3f803f80\n\t" \
  "v_and_b32 v222, 16, v225\n\t" \
  "v_cndmask_b32 v223, 0, v220, vcc\n\t" \
  "v_and_b32 v220, 0x70, v224\n\t" \
  "v_lshl_or_b32 v222, v222, 8, v220\n\t" \
  "v_and_b32 v220, 32, v225\n\t" \
  "v_lshl_or_b32 v222, v220, 26, v222\n\t" \
  "v_and_or_b32 v225, v225, 8, v222\n\t" \
  "v_mov_b32 v220, 0\n\t" \
  "v_mov_b32 v221, 0\n\t" \
  "v_mov_b32 v222, 0xdddddddd\n\t" \
  "ds_write_b32 v213, v220\n\t" \
  "s_add_u32 s60, s60, 524288\n\t" \
  "s_add_u32 s61, s60, 4096\n\t" \
  "s_mul_i32 s62, %[w], 14336\n\t" \
  "s_add_u32 s62, s62, %[lds]\n\t" \
  "s_add_u32 s62, s62, 36864\n\t" \
  "v_mov_b32 v36, v223\n\t" \
  "v_mov_b32 v37, v223\n\t" \
  "v_mov_b32 v38, v223\n\t" \
  "v_mov_b32 v39, v223\n\t" \
  "v_mov_b32 v44, v223\n\t" \
  "v_mov_b32 v45, v223\n\t" \
  "v_mov_b32 v46, v223\n\t" \
  "v_mov_b32 v47, v223\n\t" \
  "v_mov_b32 v52, v223\n\t" \
  "v_mov_b32 v53, v223\n\t" \
  "v_mov_b32 v54, v223\n\t" \
  "v_mov_b32 v55, v223\n\t" \
  "v_mov_b32 v60, v223\n\t" \
  "v_mov_b32 v61, v223\n\t" \
  "v_mov_b32 v62, v223\n\t" \
  "v_mov_b32 v63, v223\n\t" \
  "v_mov_b32 v68, v223\n\t" \
  "v_mov_b32 v69, v223\n\t" \
  "v_mov_b32 v70, v223\n\t" \
  "v_mov_b32 v71, v223\n\t" \
  "v_mov_b32 v76, v223\n\t" \
  "v_mov_b32 v77, v223\n\t" \
  "v_mov_b32 v78, v223\n\t" \
  "v_mov_b32 v79, v223\n\t" \
  "v_mov_b32 v84, v223\n\t" \
  "v_mov_b32 v85, v223\n\t" \
  "v_mov_b32 v86, v223\n\t" \
  "v_mov_b32 v87, v223\n\t" \
  "v_mov_b32 v92, v223\n\t" \
  "v_mov_b32 v93, v223\n\t" \
  "v_mov_b32 v94, v223\n\t" \
  "v_mov_b32 v95, v223\n\t" \
  "v_mov_b32 v252, -1\n\t" \
  "v_mov_b32 v253, -1\n\t" \
  "v_mov_b32 v254, -1\n\t" \
  "v_mov_b32 v255, -1\n\t" \
  "v_mov_b32 v128, %[nsh]\n\t" \
  "v_mov_b32 v129, %[nsh]\n\t" \
  "v_mov_b32 v130, %[nsh]\n\t" \
  "v_mov_b32 v131, %[nsh]\n\t" \
  "v_mov_b32 v132, %[nsh]\n\t" \
  "v_mov_b32 v133, %[nsh]\n\t" \
  "v_mov_b32 v134, %[nsh]\n\t" \
  "v_mov_b32 v135, %[nsh]\n\t" \
  "v_mov_b32 v136, %[nsh]\n\t" \
  "v_mov_b32 v137, %[nsh]\n\t" \
  "v_mov_b32 v138, %[nsh]\n\t" \
  "v_mov_b32 v139, %[nsh]\n\t" \
  "v_mov_b32 v140, %[nsh]\n\t" \
  "v_mov_b32 v141, %[nsh]\n\t" \
  "v_mov_b32 v142, %[nsh]\n\t" \
  "v_mov_b32 v143, %[nsh]\n\t" \
  "s_waitcnt vmcnt(1)\n\t" \
  "v_mfma_f32_32x32x16_bf16 v[160:175], v[0:3], v[208:211], v[128:143]\n\t" \
  "ds_write_b128 v212, v[176:179]\n\t" \
  "buffer_load_dwordx4 v[4:7], v224, %[rs], s76 offen offset:1024 sc1\n\t" \
  "buffer_load_dwordx4 v[40:43], v224, %[rs], s78 offen offset:1024 sc1\n\t" \
  "buffer_load_dwordx4 v[8:11], v224, %[rs], s76 offen offset:2048 sc1\n\t" \
  "buffer_load_dwordx4 v[48:51], v224, %[rs], s78 offen offset:2048 sc1\n\t" \
  "v_dot2c_f32_bf16 v146, v176, v176\n\t" \
  "v_dot2c_f32_bf16 v146, v177, v177\n\t" \
  "v_dot2c_f32_bf16 v146, v178, v178\n\t" \
  "v_dot2c_f32_bf16 v146, v179, v179\n\t" \
  "s_nop 2\n\t" \
  "v_mov_b32 v145, v146\n\t" \
  "s_nop 1\n\t" \
  "v_permlane32_swap_b32 v146, v145\n\t" \
  "s_mov_b32 exec_hi, 0\n\t" \
  "v_add_f32 v144, v146, v145\n\t" \
  "s_mov_b32 exec_hi, -1\n\t" \
  "s_nop 1\n\t" \
  "v_max_u32_dpp v144, v144, v144 quad_perm:[1,0,3,2] row_mask:0xf bank_mask:0xf\n\t" \
  "s_nop 1\n\t" \
  "v_max_u32_dpp v144, v144, v144 quad_perm:[2,3,0,1] row_mask:0xf bank_mask:0xf\n\t" \
  "s_nop 1\n\t" \
  "v_max_u32_dpp v144, v144, v144 row_half_mirror row_mask:0xf bank_mask:0xf\n\t" \
  "s_nop 1\n\t" \
  "v_max_u32_dpp v144, v144, v144 row_mirror row_mask:0xf bank_mask:0xf\n\t" \
  "s_nop 1\n\t" \
  "ds_write_b32 v214, v144\n\t" \
  "s_waitcnt lgkmcnt(0)\n\t" \
  "s_barrier\n\t" \
  "s_setprio 3\n\t" \
  "ds_read_b32 v215, v215\n\t" \
  "v_mfma_f32_32x32x8_bf16 v[144:159], v[220:221], v[220:221], 0\n\t" \
  "buffer_load_dwordx4 v[12:15], v224, %[rs], s76 offen offset:3072 sc1\n\t" \
  "v_exp_f32 v160, v160\n\t" \
  "buffer_load_dwordx4 v[56:59], v224, %[rs], s78 offen offset:3072 sc1\n\t" \
  "v_exp_f32 v161, v161\n\t" \
  "buffer_load_dwordx4 v[16:19], v224, %[rs], s77 offen offset:0 sc1\n\t" \
  "v_exp_f32 v162, v162\n\t" \
  "buffer_load_dwordx4 v[64:67], v224, %[rs], s79 offen offset:0 sc1\n\t" \
  "v_exp_f32 v163, v163\n\t" \
  "v_exp_f32 v164, v164\n\t" \
  "s_waitcnt vmcnt(7)\n\t" \
  "v_mfma_f32_32x32x16_bf16 v[176:191], v[4:7], v[208:211], v[128:143]\n\t" \
  "v_exp_f32 v165, v165\n\t" \
  "v_exp_f32 v166, v166\n\t" \
  "v_permlane16_swap_b32 v32, v36\n\t" \
  "v_permlane16_swap_b32 v33, v37\n\t" \
  "v_permlane16_swap_b32 v34, v38\n\t" \
  "v_permlane16_swap_b32 v35, v39\n\t" \
  "v_exp_f32 v167, v167\n\t" \
  "v_exp_f32 v168, v168\n\t" \
  "v_exp_f32 v169, v169\n\t" \
  "v_smfmac_f32_32x32x32_bf16 v[144:159], v[32:35], v[160:167], v222\n\t" \
  "v_exp_f32 v170, v170\n\t" \
  "s_waitcnt lgkmcnt(0)\n\t" \
  "v_max_u32_dpp v215, v215, v215 quad_perm:[1,0,3,2] row_mask:0xf bank_mask:0xf\n\t" \
  "v_exp_f32 v171, v171\n\t" \
  "v_exp_f32 v172, v172\n\t" \
  "v_max_u32_dpp v215, v215, v215 quad_perm:[2,3,0,1] row_mask:0xf bank_mask:0xf\n\t" \
  "v_exp_f32 v173, v173\n\t" \
  "v_exp_f32 v174, v174\n\t" \
  "v_max_u32_dpp v215, v215, v215 row_half_mirror row_mask:0xf bank_mask:0xf\n\t" \
  "v_exp_f32 v175, v175\n\t" \
  "buffer_load_dwordx4 v[20:23], v224, %[rs], s77 offen offset:1024 sc1\n\t" \
  "v_exp_f32 v176, v176\n\t" \
  "buffer_load_dwordx4 v[72:75], v224, %[rs], s79 offen offset:1024 sc1\n\t" \
  "v_max_u32_dpp v215, v215, v215 row_mirror row_mask:0xf bank_mask:0xf\n\t" \
  "v_exp_f32 v177, v177\n\t" \
  "v_smfmac_f32_32x32x32_bf16 v[144:159], v[36:39], v[168:175], v222\n\t" \
  "buffer_load_dwordx4 v[24:27], v224, %[rs], s77 offen offset:2048 sc1\n\t" \
  "v_exp_f32 v178, v178\n\t" \
  "buffer_load_dwordx4 v[80:83], v224, %[rs], s79 offen offset:2048 sc1\n\t" \
  "v_readlane_b32 s68, v215, 16\n\t" \
  "v_exp_f32 v179, v179\n\t" \
  "v_exp_f32 v180, v180\n\t" \
  "s_waitcnt vmcnt(9)\n\t" \
  "v_mfma_f32_32x32x16_bf16 v[160:175], v[8:11], v[208:211], v[128:143]\n\t" \
  "v_exp_f32 v181, v181\n\t" \
  "v_exp_f32 v182, v182\n\t" \
  "v_permlane16_swap_b32 v40, v44\n\t" \
  "v_permlane16_swap_b32 v41, v45\n\t" \
  "v_permlane16_swap_b32 v42, v46\n\t" \
  "v_permlane16_swap_b32 v43, v47\n\t" \
  "v_exp_f32 v183, v183\n\t" \
  "v_exp_f32 v184, v184\n\t" \
  "v_mul_f32 v215, s68, v215\n\t" \
  "v_cmp_ge_f32 vcc, %[lim2], v215\n\t" \
  "v_exp_f32 v185, v185\n\t" \
  "v_smfmac_f32_32x32x32_bf16 v[144:159], v[40:43], v[176:183], v222\n\t" \
  "v_exp_f32 v186, v186\n\t" \
  "v_exp_f32 v187, v187\n\t" \
  "s_and_b32 %[fast], vcc_lo, 1\n\t" \
  "s_cbranch_scc0 LBAD%=\n\t" \
  "v_exp_f32 v188, v188\n\t" \
  "v_exp_f32 v189, v189\n\t" \
  "v_exp_f32 v190, v190\n\t" \
  "v_exp_f32 v191, v191\n\t" \
  "buffer_load_dwordx4 v[28:31], v224, %[rs], s77 offen offset:3072 sc1\n\t" \
  "v_exp_f32 v160, v160\n\t" \
  "buffer_load_dwordx4 v[88:91], v224, %[rs], s79 offen offset:3072 sc1\n\t" \
  "v_exp_f32 v161, v161\n\t" \
  "v_smfmac_f32_32x32x32_bf16 v[144:159], v[44:47], v[184:191], v222\n\t" \
  "v_exp_f32 v162, v162\n\t" \
  "v_exp_f32 v163, v163\n\t" \
  "v_exp_f32 v164, v164\n\t" \
  "s_waitcnt vmcnt(9)\n\t" \
  "v_mfma_f32_32x32x16_bf16 v[176:191], v[12:15], v[208:211], v[128:143]\n\t" \
  "v_exp_f32 v165, v165\n\t" \
  "v_exp_f32 v166, v166\n\t" \
  "v_permlane16_swap_b32 v48, v52\n\t" \
  "v_permlane16_swap_b32 v49, v53\n\t" \
  "v_permlane16_swap_b32 v50, v54\n\t" \
  "v_permlane16_swap_b32 v51, v55\n\t" \
  "v_exp_f32 v167, v167\n\t" \
  "v_exp_f32 v168, v168\n\t" \
  "v_exp_f32 v169, v169\n\t" \
  "v_smfmac_f32_32x32x32_bf16 v[144:159], v[48:51], v[160:167], v222\n\t" \
  "v_exp_f32 v170, v170\n\t" \
  "v_exp_f32 v171, v171\n\t" \
  "v_exp_f32 v172, v172\n\t" \
  "v_exp_f32 v173, v173\n\t" \
  "v_exp_f32 v174, v174\n\t" \
  "v_exp_f32 v175, v175\n\t" \
  "buffer_load_dwordx2 v[96:97], v225, %[rx], %[sx] offen offset:0 sc1\n\t" \
  "v_exp_f32 v176, v176\n\t" \
  "buffer_load_dwordx2 v[98:99], v225, %[rx], %[sx] offen offset:1024 sc1\n\t" \
  "v_exp_f32 v177, v177\n\t" \
  "v_smfmac_f32_32x32x32_bf16 v[144:159], v[52:55], v[168:175], v222\n\t" \
  "buffer_load_dwordx2 v[100:101], v225, %[rx], %[sx] offen offset:2048 sc1\n\t" \
  "v_exp_f32 v178, v178\n\t" \
  "buffer_load_dwordx2 v[102:103], v225, %[rx], %[sx] offen offset:3072 sc1\n\t" \
  "v_exp_f32 v179, v179\n\t" \
  "v_exp_f32 v180, v180\n\t" \
  "s_waitcnt vmcnt(11)\n\t" \
  "v_mfma_f32_32x32x16_bf16 v[160:175], v[16:19], v[208:211], v[128:143]\n\t" \
  "v_exp_f32 v181, v181\n\t" \
  "v_exp_f32 v182, v182\n\t" \
  "v_permlane16_swap_b32 v56, v60\n\t" \
  "v_permlane16_swap_b32 v57, v61\n\t" \
  "v_permlane16_swap_b32 v58, v62\n\t" \
  "v_permlane16_swap_b32 v59, v63\n\t" \
  "v_exp_f32 v183, v183\n\t" \
  "v_exp_f32 v184, v184\n\t" \
  "v_exp_f32 v185, v185\n\t" \
  "v_smfmac_f32_32x32x32_bf16 v[144:159], v[56:59], v[176:183], v222\n\t" \
  "v_exp_f32 v186, v186\n\t" \
  "v_exp_f32 v187, v187\n\t" \
  "v_exp_f32 v188, v188\n\t" \
  "v_exp_f32 v189, v189\n\t" \
  "v_exp_f32 v190, v190\n\t" \
  "v_exp_f32 v191, v191\n\t" \
  "v_exp_f32 v160, v160\n\t" \
  "v_exp_f32 v161, v161\n\t" \
  "v_smfmac_f32_32x32x32_bf16 v[144:159], v[60:63], v[184:191], v222\n\t" \
  "v_exp_f32 v162, v162\n\t" \
  "v_exp_f32 v163, v163\n\t" \
  "v_exp_f32 v164, v164\n\t" \
  "s_waitcnt vmcnt(9)\n\t" \
  "v_mfma_f32_32x32x16_bf16 v[176:191], v[20:23], v[208:211], v[128:143]\n\t" \
  "v_exp_f32 v165, v165\n\t" \
  "v_exp_f32 v166, v166\n\t" \
  "v_permlane16_swap_b32 v64, v68\n\t" \
  "v_permlane16_swap_b32 v65, v69\n\t" \
  "v_permlane16_swap_b32 v66, v70\n\t" \
  "v_permlane16_swap_b32 v67, v71\n\t" \
  "v_exp_f32 v167, v167\n\t" \
  "v_exp_f32 v168, v168\n\t" \
  "v_exp_f32 v169, v169\n\t" \
  "v_smfmac_f32_32x32x32_bf16 v[144:159], v[64:67], v[160:167], v222\n\t" \
  "v_exp_f32 v170, v170\n\t" \
  "v_exp_f32 v171, v171\n\t" \
  "v_exp_f32 v172, v172\n\t" \
  "v_exp_f32 v173, v173\n\t" \
  "v_exp_f32 v174, v174\n\t" \
  "v_exp_f32 v175, v175\n\t" \
  "v_exp_f32 v176, v176\n\t" \
  "v_exp_f32 v177, v177\n\t" \
  "v_smfmac_f32_32x32x32_bf16 v[144:159], v[68:71], v[168:175], v222\n\t" \
  "v_exp_f32 v178, v178\n\t" \
  "v_exp_f32 v179, v179\n\t" \
  "v_exp_f32 v180, v180\n\t" \
  "s_waitcnt vmcnt(7)\n\t" \
  "v_mfma_f32_32x32x16_bf16 v[160:175], v[24:27], v[208:211], v[128:143]\n\t" \
  "v_exp_f32 v181, v181\n\t" \
  "v_exp_f32 v182, v182\n\t" \
  "v_permlane16_swap_b32 v72, v76\n\t" \
  "v_permlane16_swap_b32 v73, v77\n\t" \
  "v_permlane16_swap_b32 v74, v78\n\t" \
  "v_permlane16_swap_b32 v75, v79\n\t" \
  "v_exp_f32 v183, v183\n\t" \
  "v_exp_f32 v184, v184\n\t" \
  "v_exp_f32 v185, v185\n\t" \
  "v_smfmac_f32_32x32x32_bf16 v[144:159], v[72:75], v[176:183], v222\n\t" \
  "v_exp_f32 v186, v186\n\t" \
  "v_exp_f32 v187, v187\n\t" \
  "v_exp_f32 v188, v188\n\t" \
  "v_exp_f32 v189, v189\n\t" \
  "v_exp_f32 v190, v190\n\t" \
  "v_exp_f32 v191, v191\n\t" \
  "v_exp_f32 v160, v160\n\t" \
  "v_exp_f32 v161, v161\n\t" \
  "v_smfmac_f32_32x32x32_bf16 v[144:159], v[76:79], v[184:191], v222\n\t" \
  "v_exp_f32 v162, v162\n\t" \
  "v_exp_f32 v163, v163\n\t" \
  "v_exp_f32 v164, v164\n\t" \
  "s_waitcnt vmcnt(5)\n\t" \
  "v_mfma_f32_32x32x16_bf16 v[176:191], v[28:31], v[208:211], v[128:143]\n\t" \
  "ds_read_b128 v[208:211], v226 offset:1024\n\t" \
  "v_exp_f32 v165, v165\n\t" \
  "v_exp_f32 v166, v166\n\t" \
  "v_permlane16_swap_b32 v80, v84\n\t" \
  "v_permlane16_swap_b32 v81, v85\n\t" \
  "v_permlane16_swap_b32 v82, v86\n\t" \
  "v_permlane16_swap_b32 v83, v87\n\t" \
  "v_exp_f32 v167, v167\n\t" \
  "v_exp_f32 v168, v168\n\t" \
  "v_exp_f32 v169, v169\n\t" \
  "v_smfmac_f32_32x32x32_bf16 v[144:159], v[80:83], v[160:167], v222\n\t" \
  "v_exp_f32 v170, v170\n\t" \
  "v_exp_f32 v171, v171\n\t" \
  "v_exp_f32 v172, v172\n\t" \
  "v_exp_f32 v173, v173\n\t" \
  "v_exp_f32 v174, v174\n\t" \
  "v_exp_f32 v175, v175\n\t" \
  "v_exp_f32 v176, v176\n\t" \
  "v_exp_f32 v177, v177\n\t" \
  "v_smfmac_f32_32x32x32_bf16 v[144:159], v[84:87], v[168:175], v222\n\t" \
  "v_exp_f32 v178, v178\n\t" \
  "v_exp_f32 v179, v179\n\t" \
  "v_exp_f32 v180, v180\n\t" \
  "s_waitcnt vmcnt(0)\n\t" \
  "s_waitcnt lgkmcnt(0)\n\t" \
  "v_mfma_f32_32x32x16_bf16 v[160:175], v[0:3], v[208:211], v[128:143]\n\t" \
  "v_exp_f32 v181, v181\n\t" \
  "v_exp_f32 v182, v182\n\t" \
  "v_permlane16_swap_b32 v88, v92\n\t" \
  "v_permlane16_swap_b32 v89, v93\n\t" \
  "v_permlane16_swap_b32 v90, v94\n\t" \
  "v_permlane16_swap_b32 v91, v95\n\t" \
  "v_exp_f32 v183, v183\n\t" \
  "v_exp_f32 v184, v184\n\t" \
  "v_exp_f32 v185, v185\n\t" \
  "v_smfmac_f32_32x32x32_bf16 v[144:159], v[88:91], v[176:183], v222\n\t" \
  "v_exp_f32 v186, v186\n\t" \
  "v_exp_f32 v187, v187\n\t" \
  "v_exp_f32 v188, v188\n\t" \
  "v_exp_f32 v189, v189\n\t" \
  "v_exp_f32 v190, v190\n\t" \
  "v_exp_f32 v191, v191\n\t" \
  "s_mov_b32 m0, s59\n\t" \
  "s_cmp_eq_u32 s72, 0\n\t" \
  "v_smfmac_f32_32x32x32_bf16 v[144:159], v[92:95], v[184:191], v222\n\t" \
  "s_nop 11\n\t" \
  "ds_write_b128 v228, v[144:147] offset:0\n\t" \
  "ds_write_b128 v228, v[148:151] offset:1024\n\t" \
  "ds_write_addtid_b32 v152 offset:2048\n\t" \
  "s_mov_b64 exec, 1\n\t" \
  "ds_sub_u32 v230, v252 offset:0\n\t" \
  "s_mov_b64 exec, -1\n\t" \
  "v_mfma_f32_32x32x8_bf16 v[144:159], v[220:221], v[220:221], 0\n\t" \
  "v_exp_f32 v160, v160\n\t" \
  "v_exp_f32 v161, v161\n\t" \
  "v_exp_f32 v162, v162\n\t" \
  "v_exp_f32 v163, v163\n\t" \
  "v_exp_f32 v164, v164\n\t" \
  "v_mfma_f32_32x32x16_bf16 v[176:191], v[4:7], v[208:211], v[128:143]\n\t" \
  "v_exp_f32 v165, v165\n\t" \
  "s_add_u32 s71, %[swb], 7168\n\t" \
  "s_add_u32 s64, %[swa], 7168\n\t" \
  "s_add_u32 s77, %[swb], 4096\n\t" \
  "s_add_u32 s63, %[swa], 4096\n\t" \
  "v_exp_f32 v166, v166\n\t" \
  "buffer_load_dwordx4 v[232:235], v224, %[rs], s64 offen offset:0\n\t" \
  "v_exp_f32 v167, v167\n\t" \
  "v_exp_f32 v168, v168\n\t" \
  "buffer_load_dwordx4 v[236:239], v224, %[rs], s71 offen offset:0\n\t" \
  "v_exp_f32 v169, v169\n\t" \
  "v_smfmac_f32_32x32x32_bf16 v[144:159], v[32:35], v[160:167], v222\n\t" \
  "v_exp_f32 v170, v170\n\t" \
  "v_exp_f32 v171, v171\n\t" \
  "v_lshlrev_b32 v192, 16, v96\n\t" \
  "v_and_b32 v196, 0xffff0000, v96\n\t" \
  "v_exp_f32 v172, v172\n\t" \
  "v_exp_f32 v173, v173\n\t" \
  "v_lshlrev_b32 v200, 16, v97\n\t" \
  "v_and_b32 v204, 0xffff0000, v97\n\t" \
  "v_exp_f32 v174, v174\n\t" \
  "v_exp_f32 v175, v175\n\t" \
  "v_exp_f32 v176, v176\n\t" \
  "v_exp_f32 v177, v177\n\t" \
  "v_smfmac_f32_32x32x32_bf16 v[144:159], v[36:39], v[168:175], v222\n\t" \
  "v_exp_f32 v178, v178\n\t" \
  "v_exp_f32 v179, v179\n\t" \
  "v_exp_f32 v180, v180\n\t" \
  "v_mfma_f32_32x32x16_bf16 v[160:175], v[8:11], v[208:211], v[128:143]\n\t" \
  "v_exp_f32 v181, v181\n\t" \
  "v_exp_f32 v182, v182\n\t" \
  "buffer_load_dwordx4 v[240:243], v224, %[rs], s64 offen offset:1024\n\t" \
  "v_exp_f32 v183, v183\n\t" \
  "v_exp_f32 v184, v184\n\t" \
  "buffer_load_dwordx4 v[244:247], v224, %[rs], s71 offen offset:1024\n\t" \
  "v_exp_f32 v185, v185\n\t" \
  "v_smfmac_f32_32x32x32_bf16 v[144:159], v[40:43], v[176:183], v222\n\t" \
  "v_exp_f32 v186, v186\n\t" \
  "v_exp_f32 v187, v187\n\t" \
  "v_lshlrev_b32 v193, 16, v98\n\t" \
  "v_and_b32 v197, 0xffff0000, v98\n\t" \
  "v_exp_f32 v188, v188\n\t" \
  "v_exp_f32 v189, v189\n\t" \
  "v_lshlrev_b32 v201, 16, v99\n\t" \
  "v_and_b32 v205, 0xffff0000, v99\n\t" \
  "v_exp_f32 v190, v190\n\t" \
  "v_exp_f32 v191, v191\n\t" \
  "v_exp_f32 v160, v160\n\t" \
  "v_exp_f32 v161, v161\n\t" \
  "v_smfmac_f32_32x32x32_bf16 v[144:159], v[44:47], v[184:191], v222\n\t" \
  "v_exp_f32 v162, v162\n\t" \
  "v_exp_f32 v163, v163\n\t" \
  "v_exp_f32 v164, v164\n\t" \
  "v_mfma_f32_32x32x16_bf16 v[176:191], v[12:15], v[208:211], v[128:143]\n\t" \
  "v_exp_f32 v165, v165\n\t" \
  "v_exp_f32 v166, v166\n\t" \
  "buffer_load_dwordx4 v[248:251], v224, %[rs], s64 offen offset:2048\n\t" \
  "v_exp_f32 v167, v167\n\t" \
  "v_exp_f32 v168, v168\n\t" \
  "v_exp_f32 v169, v169\n\t" \
  "v_smfmac_f32_32x32x32_bf16 v[144:159], v[48:51], v[160:167], v222\n\t" \
  "v_exp_f32 v170, v170\n\t" \
  "s_add_u32 m0, s62, 0\n\t" \
  "v_exp_f32 v171, v171\n\t" \
  "v_lshlrev_b32 v194, 16, v100\n\t" \
  "v_and_b32 v198, 0xffff0000, v100\n\t" \
  "buffer_load_dwordx4 v224, %[rs], %[swa] offen offset:0 lds\n\t" \
  "v_exp_f32 v172, v172\n\t" \
  "v_exp_f32 v173, v173\n\t" \
  "v_lshlrev_b32 v202, 16, v101\n\t" \
  "v_and_b32 v206, 0xffff0000, v101\n\t" \
  "v_exp_f32 v174, v174\n\t" \
  "v_exp_f32 v175, v175\n\t" \
  "v_exp_f32 v176, v176\n\t" \
  "v_exp_f32 v177, v177\n\t" \
  "v_smfmac_f32_32x32x32_bf16 v[144:159], v[52:55], v[168:175], v222\n\t" \
  "v_exp_f32 v178, v178\n\t" \
  "s_add_u32 m0, s62, 1024\n\t" \
  "v_exp_f32 v179, v179\n\t" \
  "buffer_load_dwordx4 v224, %[rs], %[swb] offen offset:0 lds\n\t" \
  "v_exp_f32 v180, v180\n\t" \
  "v_mfma_f32_32x32x16_bf16 v[160:175], v[16:19], v[208:211], v[128:143]\n\t" \
  "v_exp_f32 v181, v181\n\t" \
  "v_exp_f32 v182, v182\n\t" \
  "s_add_u32 m0, s62, 1024\n\t" \
  "v_exp_f32 v183, v183\n\t" \
  "buffer_load_dwordx4 v224, %[rs], %[swa] offen offset:1024 lds\n\t" \
  "v_exp_f32 v184, v184\n\t" \
  "v_exp_f32 v185, v185\n\t" \
  "v_smfmac_f32_32x32x32_bf16 v[144:159], v[56:59], v[176:183], v222\n\t" \
  "v_exp_f32 v186, v186\n\t" \
  "v_exp_f32 v187, v187\n\t" \
  "v_lshlrev_b32 v195, 16, v102\n\t" \
  "v_and_b32 v199, 0xffff0000, v102\n\t" \
  "s_add_u32 m0, s62, 2048\n\t" \
  "v_exp_f32 v188, v188\n\t" \
  "buffer_load_dwordx4 v224, %[rs], %[swb] offen offset:1024 lds\n\t" \
  "v_exp_f32 v189, v189\n\t" \
  "v_lshlrev_b32 v203, 16, v103\n\t" \
  "v_and_b32 v207, 0xffff0000, v103\n\t" \
  "v_exp_f32 v190, v190\n\t" \
  "v_exp_f32 v191, v191\n\t" \
  "v_exp_f32 v160, v160\n\t" \
  "v_exp_f32 v161, v161\n\t" \
  "v_smfmac_f32_32x32x32_bf16 v[144:159], v[60:63], v[184:191], v222\n\t" \
  "v_exp_f32 v162, v162\n\t" \
  "v_exp_f32 v163, v163\n\t" \
  "v_exp_f32 v164, v164\n\t" \
  "v_mfma_f32_32x32x16_bf16 v[176:191], v[20:23], v[208:211], v[128:143]\n\t" \
  "v_exp_f32 v165, v165\n\t" \
  "v_exp_f32 v166, v166\n\t" \
  "buffer_load_dwordx4 v[96:99], v224, %[rs], s60 offen offset:0\n\t" \
  "v_exp_f32 v167, v167\n\t" \
  "v_exp_f32 v168, v168\n\t" \
  "buffer_load_dwordx4 v[100:103], v224, %[rs], s60 offen offset:1024\n\t" \
  "v_exp_f32 v169, v169\n\t" \
  "v_smfmac_f32_32x32x32_bf16 v[144:159], v[64:67], v[160:167], v222\n\t" \
  "v_exp_f32 v170, v170\n\t" \
  "v_exp_f32 v171, v171\n\t" \
  "v_exp_f32 v172, v172\n\t" \
  "v_exp_f32 v173, v173\n\t" \
  "v_exp_f32 v174, v174\n\t" \
  "v_exp_f32 v175, v175\n\t" \
  "v_exp_f32 v176, v176\n\t" \
  "v_exp_f32 v177, v177\n\t" \
  "v_smfmac_f32_32x32x32_bf16 v[144:159], v[68:71], v[168:175], v222\n\t" \
  "v_exp_f32 v178, v178\n\t" \
  "v_exp_f32 v179, v179\n\t" \
  "v_exp_f32 v180, v180\n\t" \
  "v_mfma_f32_32x32x16_bf16 v[160:175], v[24:27], v[208:211], v[128:143]\n\t" \
  "v_exp_f32 v181, v181\n\t" \
  "v_exp_f32 v182, v182\n\t" \
  "buffer_load_dwordx4 v[104:107], v224, %[rs], s60 offen offset:2048\n\t" \
  "v_exp_f32 v183, v183\n\t" \
  "v_exp_f32 v184, v184\n\t" \
  "buffer_load_dwordx4 v[108:111], v224, %[rs], s60 offen offset:3072\n\t" \
  "v_exp_f32 v185, v185\n\t" \
  "v_smfmac_f32_32x32x32_bf16 v[144:159], v[72:75], v[176:183], v222\n\t" \
  "v_exp_f32 v186, v186\n\t" \
  "v_exp_f32 v187, v187\n\t" \
  "v_exp_f32 v188, v188\n\t" \
  "v_exp_f32 v189, v189\n\t" \
  "v_exp_f32 v190, v190\n\t" \
  "v_exp_f32 v191, v191\n\t" \
  "v_exp_f32 v160, v160\n\t" \
  "v_exp_f32 v161, v161\n\t" \
  "v_smfmac_f32_32x32x32_bf16 v[144:159], v[76:79], v[184:191], v222\n\t" \
  "v_exp_f32 v162, v162\n\t" \
  "v_exp_f32 v163, v163\n\t" \
  "v_exp_f32 v164, v164\n\t" \
  "v_mfma_f32_32x32x16_bf16 v[176:191], v[28:31], v[208:211], v[128:143]\n\t" \
  "ds_read_b128 v[208:211], v226 offset:2048\n\t" \
  "v_exp_f32 v165, v165\n\t" \
  "v_exp_f32 v166, v166\n\t" \
  "buffer_load_dwordx4 v[112:115], v224, %[rs], s61 offen offset:0\n\t" \
  "v_exp_f32 v167, v167\n\t" \
  "v_exp_f32 v168, v168\n\t" \
  "buffer_load_dwordx4 v[116:119], v224, %[rs], s61 offen offset:1024\n\t" \
  "v_exp_f32 v169, v169\n\t" \
  "v_smfmac_f32_32x32x32_bf16 v[144:159], v[80:83], v[160:167], v222\n\t" \
  "v_exp_f32 v170, v170\n\t" \
  "v_exp_f32 v171, v171\n\t" \
  "v_exp_f32 v172, v172\n\t" \
  "v_exp_f32 v173, v173\n\t" \
  "v_exp_f32 v174, v174\n\t" \
  "v_exp_f32 v175, v175\n\t" \
  "v_exp_f32 v176, v176\n\t" \
  "v_exp_f32 v177, v177\n\t" \
  "v_smfmac_f32_32x32x32_bf16 v[144:159], v[84:87], v[168:175], v222\n\t" \
  "v_exp_f32 v178, v178\n\t" \
  "v_exp_f32 v179, v179\n\t" \
  "v_exp_f32 v180, v180\n\t" \
  "s_waitcnt lgkmcnt(0)\n\t" \
  "v_mfma_f32_32x32x16_bf16 v[160:175], v[0:3], v[208:211], v[128:143]\n\t" \
  "v_exp_f32 v181, v181\n\t" \
  "v_exp_f32 v182, v182\n\t" \
  "buffer_load_dwordx4 v[120:123], v224, %[rs], s61 offen offset:2048\n\t" \
  "v_exp_f32 v183, v183\n\t" \
  "v_exp_f32 v184, v184\n\t" \
  "buffer_load_dwordx4 v[124:127], v224, %[rs], s61 offen offset:3072\n\t" \
  "v_exp_f32 v185, v185\n\t" \
  "v_smfmac_f32_32x32x32_bf16 v[144:159], v[88:91], v[176:183], v222\n\t" \
  "v_exp_f32 v186, v186\n\t" \
  "v_exp_f32 v187, v187\n\t" \
  "buffer_store_dwordx4 v[252:255], v224, %[rs], %[sph] offen offset:0 sc1\n\t" \
  "v_exp_f32 v188, v188\n\t" \
  "v_exp_f32 v189, v189\n\t" \
  "v_exp_f32 v190, v190\n\t" \
  "v_exp_f32 v191, v191\n\t" \
  "s_mov_b32 m0, s59\n\t" \
  "s_cmp_eq_u32 s72, 1\n\t" \
  "v_smfmac_f32_32x32x32_bf16 v[144:159], v[92:95], v[184:191], v222\n\t" \
  "s_nop 11\n\t" \
  "ds_write_b128 v228, v[144:147] offset:18432\n\t" \
  "ds_write_b128 v228, v[148:151] offset:19456\n\t" \
  "ds_write_addtid_b32 v152 offset:20480\n\t" \
  "s_mov_b64 exec, 1\n\t" \
  "ds_sub_u32 v230, v252 offset:4\n\t" \
  "s_mov_b64 exec, -1\n\t" \
  "s_cbranch_scc1 LRED%=\n\t" \
  "LRB1%=:\n\t" \
  "s_cmp_lt_u32 %[w], 4\n\t" \
  "s_cbranch_scc0 LP2%=\n\t" \
  "s_setprio 2\n\t" \
  "LP2%=:\n\t" \
  "v_mfma_f32_32x32x8_bf16 v[144:159], v[220:221], v[220:221], 0\n\t" \
  "v_exp_f32 v160, v160\n\t" \
  "v_exp_f32 v161, v161\n\t" \
  "v_exp_f32 v162, v162\n\t" \
  "v_exp_f32 v163, v163\n\t" \
  "v_exp_f32 v164, v164\n\t" \
  "v_mfma_f32_32x32x16_bf16 v[176:191], v[4:7], v[208:211], v[128:143]\n\t" \
  "v_exp_f32 v165, v165\n\t" \
  "v_exp_f32 v166, v166\n\t" \
  "s_add_u32 m0, s62, 2048\n\t" \
  "v_exp_f32 v167, v167\n\t" \
  "buffer_load_dwordx4 v224, %[rs], %[swa] offen offset:2048 lds\n\t" \
  "v_exp_f32 v168, v168\n\t" \
  "v_exp_f32 v169, v169\n\t" \
  "v_smfmac_f32_32x32x32_bf16 v[144:159], v[32:35], v[160:167], v222\n\t" \
  "v_exp_f32 v170, v170\n\t" \
  "v_exp_f32 v171, v171\n\t" \
  "v_exp_f32 v172, v172\n\t" \
  "v_exp_f32 v173, v173\n\t" \
  "v_exp_f32 v174, v174\n\t" \
  "v_exp_f32 v175, v175\n\t" \
  "v_exp_f32 v176, v176\n\t" \
  "v_exp_f32 v177, v177\n\t" \
  "v_smfmac_f32_32x32x32_bf16 v[144:159], v[36:39], v[168:175], v222\n\t" \
  "v_exp_f32 v178, v178\n\t" \
  "v_exp_f32 v179, v179\n\t" \
  "v_exp_f32 v180, v180\n\t" \
  "v_mfma_f32_32x32x16_bf16 v[160:175], v[8:11], v[208:211], v[128:143]\n\t" \
  "v_exp_f32 v181, v181\n\t" \
  "v_exp_f32 v182, v182\n\t" \
  "s_add_u32 m0, s62, 3072\n\t" \
  "v_exp_f32 v183, v183\n\t" \
  "buffer_load_dwordx4 v224, %[rs], %[swb] offen offset:2048 lds\n\t" \
  "v_exp_f32 v184, v184\n\t" \
  "v_exp_f32 v185, v185\n\t" \
  "v_smfmac_f32_32x32x32_bf16 v[144:159], v[40:43], v[176:183], v222\n\t" \
  "v_exp_f32 v186, v186\n\t" \
  "v_exp_f32 v187, v187\n\t" \
  "v_exp_f32 v188, v188\n\t" \
  "v_exp_f32 v189, v189\n\t" \
  "v_exp_f32 v190, v190\n\t" \
  "v_exp_f32 v191, v191\n\t" \
  "v_exp_f32 v160, v160\n\t" \
  "v_exp_f32 v161, v161\n\t" \
  "v_smfmac_f32_32x32x32_bf16 v[144:159], v[44:47], v[184:191], v222\n\t" \
  "v_exp_f32 v162, v162\n\t" \
  "v_exp_f32 v163, v163\n\t" \
  "v_exp_f32 v164, v164\n\t" \
  "v_mfma_f32_32x32x16_bf16 v[176:191], v[12:15], v[208:211], v[128:143]\n\t" \
  "v_exp_f32 v165, v165\n\t" \
  "ds_read_b32 v223, v230 offset:32\n\t" \
  "v_exp_f32 v166, v166\n\t" \
  "v_exp_f32 v167, v167\n\t" \
  "v_exp_f32 v168, v168\n\t" \
  "v_exp_f32 v169, v169\n\t" \
  "v_smfmac_f32_32x32x32_bf16 v[144:159], v[48:51], v[160:167], v222\n\t" \
  "v_exp_f32 v170, v170\n\t" \
  "v_exp_f32 v171, v171\n\t" \
  "v_exp_f32 v172, v172\n\t" \
  "v_exp_f32 v173, v173\n\t" \
  "v_exp_f32 v174, v174\n\t" \
  "v_exp_f32 v175, v175\n\t" \
  "s_waitcnt lgkmcnt(0)\n\t" \
  "v_cmp_eq_u32 vcc, 0, v223\n\t" \
  "v_exp_f32 v176, v176\n\t" \
  "s_cbranch_vccnz LSPIN2%=\n\t" \
  "LBACK2%=:\n\t" \
  "ds_read_b128 v[212:215], v227 offset:0\n\t" \
  "v_exp_f32 v177, v177\n\t" \
  "v_smfmac_f32_32x32x32_bf16 v[144:159], v[52:55], v[168:175], v222\n\t" \
  "v_exp_f32 v178, v178\n\t" \
  "v_exp_f32 v179, v179\n\t" \
  "v_exp_f32 v180, v180\n\t" \
  "v_mfma_f32_32x32x16_bf16 v[160:175], v[16:19], v[208:211], v[128:143]\n\t" \
  "v_exp_f32 v181, v181\n\t" \
  "v_exp_f32 v182, v182\n\t" \
  "v_exp_f32 v183, v183\n\t" \
  "v_exp_f32 v184, v184\n\t" \
  "v_exp_f32 v185, v185\n\t" \
  "v_smfmac_f32_32x32x32_bf16 v[144:159], v[56:59], v[176:183], v222\n\t" \
  "v_exp_f32 v186, v186\n\t" \
  "v_exp_f32 v187, v187\n\t" \
  "v_exp_f32 v188, v188\n\t" \
  "s_waitcnt vmcnt(3) lgkmcnt(0)\n\t" \
  "v_smfmac_f32_16x16x64_bf16 v[192:195], v[212:215], v[96:103], v229\n\t" \
  "v_exp_f32 v189, v189\n\t" \
  "v_exp_f32 v190, v190\n\t" \
  "v_exp_f32 v191, v191\n\t" \
  "v_exp_f32 v160, v160\n\t" \
  "v_exp_f32 v161, v161\n\t" \
  "v_smfmac_f32_32x32x32_bf16 v[144:159], v[60:63], v[184:191], v222\n\t" \
  "v_exp_f32 v162, v162\n\t" \
  "v_exp_f32 v163, v163\n\t" \
  "v_exp_f32 v164, v164\n\t" \
  "v_mfma_f32_32x32x16_bf16 v[176:191], v[20:23], v[208:211], v[128:143]\n\t" \
  "v_exp_f32 v165, v165\n\t" \
  "s_add_u32 s60, s60, 65536\n\t" \
  "v_exp_f32 v166, v166\n\t" \
  "buffer_load_dwordx4 v[96:99], v224, %[rs], s60 offen offset:0\n\t" \
  "v_exp_f32 v167, v167\n\t" \
  "v_exp_f32 v168, v168\n\t" \
  "buffer_load_dwordx4 v[100:103], v224, %[rs], s60 offen offset:1024\n\t" \
  "v_exp_f32 v169, v169\n\t" \
  "v_smfmac_f32_32x32x32_bf16 v[144:159], v[64:67], v[160:167], v222\n\t" \
  "v_exp_f32 v170, v170\n\t" \
  "v_exp_f32 v171, v171\n\t" \
  "v_exp_f32 v172, v172\n\t" \
  "v_smfmac_f32_16x16x64_bf16 v[196:199], v[212:215], v[104:111], v229\n\t" \
  "v_exp_f32 v173, v173\n\t" \
  "v_exp_f32 v174, v174\n\t" \
  "v_exp_f32 v175, v175\n\t" \
  "v_exp_f32 v176, v176\n\t" \
  "v_exp_f32 v177, v177\n\t" \
  "v_smfmac_f32_32x32x32_bf16 v[144:159], v[68:71], v[168:175], v222\n\t" \
  "v_exp_f32 v178, v178\n\t" \
  "v_exp_f32 v179, v179\n\t" \
  "v_exp_f32 v180, v180\n\t" \
  "v_mfma_f32_32x32x16_bf16 v[160:175], v[24:27], v[208:211], v[128:143]\n\t" \
  "v_exp_f32 v181, v181\n\t" \
  "v_exp_f32 v182, v182\n\t" \
  "buffer_load_dwordx4 v[104:107], v224, %[rs], s60 offen offset:2048\n\t" \
  "v_exp_f32 v183, v183\n\t" \
  "v_exp_f32 v184, v184\n\t" \
  "buffer_load_dwordx4 v[108:111], v224, %[rs], s60 offen offset:3072\n\t" \
  "v_exp_f32 v185, v185\n\t" \
  "v_smfmac_f32_32x32x32_bf16 v[144:159], v[72:75], v[176:183], v222\n\t" \
  "v_exp_f32 v186, v186\n\t" \
  "v_exp_f32 v187, v187\n\t" \
  "v_exp_f32 v188, v188\n\t" \
  "v_smfmac_f32_16x16x64_bf16 v[200:203], v[212:215], v[112:119], v229\n\t" \
  "v_exp_f32 v189, v189\n\t" \
  "v_exp_f32 v190, v190\n\t" \
  "v_exp_f32 v191, v191\n\t" \
  "v_exp_f32 v160, v160\n\t" \
  "v_exp_f32 v161, v161\n\t" \
  "v_smfmac_f32_32x32x32_bf16 v[144:159], v[76:79], v[184:191], v222\n\t" \
  "v_exp_f32 v162, v162\n\t" \
  "v_exp_f32 v163, v163\n\t" \
  "v_exp_f32 v164, v164\n\t" \
  "v_mfma_f32_32x32x16_bf16 v[176:191], v[28:31], v[208:211], v[128:143]\n\t" \
  "ds_read_b128 v[208:211], v226 offset:3072\n\t" \
  "v_exp_f32 v165, v165\n\t" \
  "s_add_u32 s61, s61, 65536\n\t" \
  "v_exp_f32 v166, v166\n\t" \
  "buffer_load_dwordx4 v[112:115], v224, %[rs], s61 offen offset:0\n\t" \
  "v_exp_f32 v167, v167\n\t" \
  "v_exp_f32 v168, v168\n\t" \
  "buffer_load_dwordx4 v[116:119], v224, %[rs], s61 offen offset:1024\n\t" \
  "v_exp_f32 v169, v169\n\t" \
  "v_smfmac_f32_32x32x32_bf16 v[144:159], v[80:83], v[160:167], v222\n\t" \
  "v_exp_f32 v170, v170\n\t" \
  "v_exp_f32 v171, v171\n\t" \
  "v_exp_f32 v172, v172\n\t" \
  "v_smfmac_f32_16x16x64_bf16 v[204:207], v[212:215], v[120:127], v229\n\t" \
  "v_exp_f32 v173, v173\n\t" \
  "v_exp_f32 v174, v174\n\t" \
  "v_exp_f32 v175, v175\n\t" \
  "v_exp_f32 v176, v176\n\t" \
  "v_exp_f32 v177, v177\n\t" \
  "v_smfmac_f32_32x32x32_bf16 v[144:159], v[84:87], v[168:175], v222\n\t" \
  "v_exp_f32 v178, v178\n\t" \
  "v_exp_f32 v179, v179\n\t" \
  "v_exp_f32 v180, v180\n\t" \
  "s_waitcnt lgkmcnt(0)\n\t" \
  "v_mfma_f32_32x32x16_bf16 v[160:175], v[0:3], v[208:211], v[128:143]\n\t" \
  "v_exp_f32 v181, v181\n\t" \
  "v_exp_f32 v182, v182\n\t" \
  "buffer_load_dwordx4 v[120:123], v224, %[rs], s61 offen offset:2048\n\t" \
  "v_exp_f32 v183, v183\n\t" \
  "v_exp_f32 v184, v184\n\t" \
  "buffer_load_dwordx4 v[124:127], v224, %[rs], s61 offen offset:3072\n\t" \
  "v_exp_f32 v185, v185\n\t" \
  "v_smfmac_f32_32x32x32_bf16 v[144:159], v[88:91], v[176:183], v222\n\t" \
  "v_exp_f32 v186, v186\n\t" \
  "v_exp_f32 v187, v187\n\t" \
  "buffer_store_dwordx4 v[252:255], v224, %[rs], %[sph] offen offset:1024 sc1\n\t" \
  "v_exp_f32 v188, v188\n\t" \
  "v_exp_f32 v189, v189\n\t" \
  "v_exp_f32 v190, v190\n\t" \
  "v_exp_f32 v191, v191\n\t" \
  "s_mov_b32 m0, s59\n\t" \
  "s_cmp_eq_u32 s72, 2\n\t" \
  "v_smfmac_f32_32x32x32_bf16 v[144:159], v[92:95], v[184:191], v222\n\t" \
  "s_nop 11\n\t" \
  "ds_write_b128 v228, v[144:147] offset:0\n\t" \
  "ds_write_b128 v228, v[148:151] offset:1024\n\t" \
  "ds_write_addtid_b32 v152 offset:2048\n\t" \
  "s_mov_b64 exec, 1\n\t" \
  "ds_sub_u32 v230, v252 offset:8\n\t" \
  "s_mov_b64 exec, -1\n\t" \
  "s_cbranch_scc1 LRED%=\n\t" \
  "LRB2%=:\n\t" \
  "s_setprio 2\n\t" \
  "v_mfma_f32_32x32x8_bf16 v[144:159], v[220:221], v[220:221], 0\n\t" \
  "v_exp_f32 v160, v160\n\t" \
  "v_exp_f32 v161, v161\n\t" \
  "v_exp_f32 v162, v162\n\t" \
  "v_exp_f32 v163, v163\n\t" \
  "v_exp_f32 v164, v164\n\t" \
  "v_mfma_f32_32x32x16_bf16 v[176:191], v[4:7], v[208:211], v[128:143]\n\t" \
  "v_exp_f32 v165, v165\n\t" \
  "v_exp_f32 v166, v166\n\t" \
  "s_add_u32 m0, s62, 3072\n\t" \
  "v_exp_f32 v167, v167\n\t" \
  "buffer_load_dwordx4 v224, %[rs], %[swa] offen offset:3072 lds\n\t" \
  "v_exp_f32 v168, v168\n\t" \
  "v_exp_f32 v169, v169\n\t" \
  "v_smfmac_f32_32x32x32_bf16 v[144:159], v[32:35], v[160:167], v222\n\t" \
  "v_exp_f32 v170, v170\n\t" \
  "v_exp_f32 v171, v171\n\t" \
  "v_exp_f32 v172, v172\n\t" \
  "v_exp_f32 v173, v173\n\t" \
  "v_exp_f32 v174, v174\n\t" \
  "v_exp_f32 v175, v175\n\t" \
  "v_exp_f32 v176, v176\n\t" \
  "v_exp_f32 v177, v177\n\t" \
  "v_smfmac_f32_32x32x32_bf16 v[144:159], v[36:39], v[168:175], v222\n\t" \
  "v_exp_f32 v178, v178\n\t" \
  "v_exp_f32 v179, v179\n\t" \
  "v_exp_f32 v180, v180\n\t" \
  "v_mfma_f32_32x32x16_bf16 v[160:175], v[8:11], v[208:211], v[128:143]\n\t" \
  "v_exp_f32 v181, v181\n\t" \
  "v_exp_f32 v182, v182\n\t" \
  "s_add_u32 m0, s62, 4096\n\t" \
  "v_exp_f32 v183, v183\n\t" \
  "buffer_load_dwordx4 v224, %[rs], %[swb] offen offset:3072 lds\n\t" \
  "v_exp_f32 v184, v184\n\t" \
  "v_exp_f32 v185, v185\n\t" \
  "v_smfmac_f32_32x32x32_bf16 v[144:159], v[40:43], v[176:183], v222\n\t" \
  "v_exp_f32 v186, v186\n\t" \
  "v_exp_f32 v187, v187\n\t" \
  "v_exp_f32 v188, v188\n\t" \
  "v_exp_f32 v189, v189\n\t" \
  "v_exp_f32 v190, v190\n\t" \
  "v_exp_f32 v191, v191\n\t" \
  "v_exp_f32 v160, v160\n\t" \
  "v_exp_f32 v161, v161\n\t" \
  "v_smfmac_f32_32x32x32_bf16 v[144:159], v[44:47], v[184:191], v222\n\t" \
  "v_exp_f32 v162, v162\n\t" \
  "v_exp_f32 v163, v163\n\t" \
  "v_exp_f32 v164, v164\n\t" \
  "v_mfma_f32_32x32x16_bf16 v[176:191], v[12:15], v[208:211], v[128:143]\n\t" \
  "v_exp_f32 v165, v165\n\t" \
  "ds_read_b32 v223, v230 offset:36\n\t" \
  "v_exp_f32 v166, v166\n\t" \
  "v_exp_f32 v167, v167\n\t" \
  "v_exp_f32 v168, v168\n\t" \
  "v_exp_f32 v169, v169\n\t" \
  "v_smfmac_f32_32x32x32_bf16 v[144:159], v[48:51], v[160:167], v222\n\t" \
  "v_exp_f32 v170, v170\n\t" \
  "v_exp_f32 v171, v171\n\t" \
  "v_exp_f32 v172, v172\n\t" \
  "v_exp_f32 v173, v173\n\t" \
  "v_exp_f32 v174, v174\n\t" \
  "v_exp_f32 v175, v175\n\t" \
  "s_waitcnt lgkmcnt(0)\n\t" \
  "v_cmp_eq_u32 vcc, 0, v223\n\t" \
  "v_exp_f32 v176, v176\n\t" \
  "s_cbranch_vccnz LSPIN3%=\n\t" \
  "LBACK3%=:\n\t" \
  "ds_read_b128 v[212:215], v227 offset:1024\n\t" \
  "v_exp_f32 v177, v177\n\t" \
  "v_smfmac_f32_32x32x32_bf16 v[144:159], v[52:55], v[168:175], v222\n\t" \
  "v_exp_f32 v178, v178\n\t" \
  "v_exp_f32 v179, v179\n\t" \
  "v_exp_f32 v180, v180\n\t" \
  "v_mfma_f32_32x32x16_bf16 v[160:175], v[16:19], v[208:211], v[128:143]\n\t" \
  "v_exp_f32 v181, v181\n\t" \
  "v_exp_f32 v182, v182\n\t" \
  "v_exp_f32 v183, v183\n\t" \
  "v_exp_f32 v184, v184\n\t" \
  "v_exp_f32 v185, v185\n\t" \
  "v_smfmac_f32_32x32x32_bf16 v[144:159], v[56:59], v[176:183], v222\n\t" \
  "v_exp_f32 v186, v186\n\t" \
  "v_exp_f32 v187, v187\n\t" \
  "v_exp_f32 v188, v188\n\t" \
  "s_waitcnt vmcnt(3) lgkmcnt(0)\n\t" \
  "v_smfmac_f32_16x16x64_bf16 v[192:195], v[212:215], v[96:103], v229\n\t" \
  "v_exp_f32 v189, v189\n\t" \
  "v_exp_f32 v190, v190\n\t" \
  "v_exp_f32 v191, v191\n\t" \
  "v_exp_f32 v160, v160\n\t" \
  "v_exp_f32 v161, v161\n\t" \
  "v_smfmac_f32_32x32x32_bf16 v[144:159], v[60:63], v[184:191], v222\n\t" \
  "v_exp_f32 v162, v162\n\t" \
  "v_exp_f32 v163, v163\n\t" \
  "v_exp_f32 v164, v164\n\t" \
  "v_mfma_f32_32x32x16_bf16 v[176:191], v[20:23], v[208:211], v[128:143]\n\t" \
  "v_exp_f32 v165, v165\n\t" \
  "s_add_u32 s60, s60, 65536\n\t" \
  "v_exp_f32 v166, v166\n\t" \
  "buffer_load_dwordx4 v[96:99], v224, %[rs], s60 offen offset:0\n\t" \
  "v_exp_f32 v167, v167\n\t" \
  "v_exp_f32 v168, v168\n\t" \
  "buffer_load_dwordx4 v[100:103], v224, %[rs], s60 offen offset:1024\n\t" \
  "v_exp_f32 v169, v169\n\t" \
  "v_smfmac_f32_32x32x32_bf16 v[144:159], v[64:67], v[160:167], v222\n\t" \
  "v_exp_f32 v170, v170\n\t" \
  "v_exp_f32 v171, v171\n\t" \
  "v_exp_f32 v172, v172\n\t" \
  "v_smfmac_f32_16x16x64_bf16 v[196:199], v[212:215], v[104:111], v229\n\t" \
  "v_exp_f32 v173, v173\n\t" \
  "v_exp_f32 v174, v174\n\t" \
  "v_exp_f32 v175, v175\n\t" \
  "v_exp_f32 v176, v176\n\t" \
  "v_exp_f32 v177, v177\n\t" \
  "v_smfmac_f32_32x32x32_bf16 v[144:159], v[68:71], v[168:175], v222\n\t" \
  "v_exp_f32 v178, v178\n\t" \
  "v_exp_f32 v179, v179\n\t" \
  "v_exp_f32 v180, v180\n\t" \
  "v_mfma_f32_32x32x16_bf16 v[160:175], v[24:27], v[208:211], v[128:143]\n\t" \
  "v_exp_f32 v181, v181\n\t" \
  "v_exp_f32 v182, v182\n\t" \
  "buffer_load_dwordx4 v[104:107], v224, %[rs], s60 offen offset:2048\n\t" \
  "v_exp_f32 v183, v183\n\t" \
  "v_exp_f32 v184, v184\n\t" \
  "buffer_load_dwordx4 v[108:111], v224, %[rs], s60 offen offset:3072\n\t" \
  "v_exp_f32 v185, v185\n\t" \
  "v_smfmac_f32_32x32x32_bf16 v[144:159], v[72:75], v[176:183], v222\n\t" \
  "v_exp_f32 v186, v186\n\t" \
  "v_exp_f32 v187, v187\n\t" \
  "v_exp_f32 v188, v188\n\t" \
  "v_smfmac_f32_16x16x64_bf16 v[200:203], v[212:215], v[112:119], v229\n\t" \
  "v_exp_f32 v189, v189\n\t" \
  "v_exp_f32 v190, v190\n\t" \
  "v_exp_f32 v191, v191\n\t" \
  "v_exp_f32 v160, v160\n\t" \
  "v_exp_f32 v161, v161\n\t" \
  "v_smfmac_f32_32x32x32_bf16 v[144:159], v[76:79], v[184:191], v222\n\t" \
  "v_exp_f32 v162, v162\n\t" \
  "v_exp_f32 v163, v163\n\t" \
  "v_exp_f32 v164, v164\n\t" \
  "v_mfma_f32_32x32x16_bf16 v[176:191], v[28:31], v[208:211], v[128:143]\n\t" \
  "ds_read_b128 v[208:211], v226 offset:4096\n\t" \
  "v_exp_f32 v165, v165\n\t" \
  "s_add_u32 s61, s61, 65536\n\t" \
  "v_exp_f32 v166, v166\n\t" \
  "buffer_load_dwordx4 v[112:115], v224, %[rs], s61 offen offset:0\n\t" \
  "v_exp_f32 v167, v167\n\t" \
  "v_exp_f32 v168, v168\n\t" \
  "buffer_load_dwordx4 v[116:119], v224, %[rs], s61 offen offset:1024\n\t" \
  "v_exp_f32 v169, v169\n\t" \
  "v_smfmac_f32_32x32x32_bf16 v[144:159], v[80:83], v[160:167], v222\n\t" \
  "v_exp_f32 v170, v170\n\t" \
  "v_exp_f32 v171, v171\n\t" \
  "v_exp_f32 v172, v172\n\t" \
  "v_smfmac_f32_16x16x64_bf16 v[204:207], v[212:215], v[120:127], v229\n\t" \
  "v_exp_f32 v173, v173\n\t" \
  "v_exp_f32 v174, v174\n\t" \
  "v_exp_f32 v175, v175\n\t" \
  "v_exp_f32 v176, v176\n\t" \
  "v_exp_f32 v177, v177\n\t" \
  "v_smfmac_f32_32x32x32_bf16 v[144:159], v[84:87], v[168:175], v222\n\t" \
  "v_exp_f32 v178, v178\n\t" \
  "v_exp_f32 v179, v179\n\t" \
  "v_exp_f32 v180, v180\n\t" \
  "s_waitcnt lgkmcnt(0)\n\t" \
  "v_mfma_f32_32x32x16_bf16 v[160:175], v[0:3], v[208:211], v[128:143]\n\t" \
  "v_exp_f32 v181, v181\n\t" \
  "v_exp_f32 v182, v182\n\t" \
  "buffer_load_dwordx4 v[120:123], v224, %[rs], s61 offen offset:2048\n\t" \
  "v_exp_f32 v183, v183\n\t" \
  "v_exp_f32 v184, v184\n\t" \
  "buffer_load_dwordx4 v[124:127], v224, %[rs], s61 offen offset:3072\n\t" \
  "v_exp_f32 v185, v185\n\t" \
  "v_smfmac_f32_32x32x32_bf16 v[144:159], v[88:91], v[176:183], v222\n\t" \
  "v_exp_f32 v186, v186\n\t" \
  "v_exp_f32 v187, v187\n\t" \
  "buffer_store_dwordx4 v[252:255], v224, %[rs], %[sph] offen offset:2048 sc1\n\t" \
  "v_exp_f32 v188, v188\n\t" \
  "v_exp_f32 v189, v189\n\t" \
  "v_exp_f32 v190, v190\n\t" \
  "v_exp_f32 v191, v191\n\t" \
  "s_mov_b32 m0, s59\n\t" \
  "s_cmp_eq_u32 s72, 3\n\t" \
  "v_smfmac_f32_32x32x32_bf16 v[144:159], v[92:95], v[184:191], v222\n\t" \
  "s_nop 11\n\t" \
  "ds_write_b128 v228, v[144:147] offset:18432\n\t" \
  "ds_write_b128 v228, v[148:151] offset:19456\n\t" \
  "ds_write_addtid_b32 v152 offset:20480\n\t" \
  "s_mov_b64 exec, 1\n\t" \
  "ds_sub_u32 v230, v252 offset:12\n\t" \
  "s_mov_b64 exec, -1\n\t" \
  "s_cbranch_scc1 LRED%=\n\t" \
  "LRB3%=:\n\t" \
  "s_cmp_lt_u32 %[w], 4\n\t" \
  "s_cbranch_scc0 LP4%=\n\t" \
  "s_setprio 1\n\t" \
  "LP4%=:\n\t" \
  "v_mfma_f32_32x32x8_bf16 v[144:159], v[220:221], v[220:221], 0\n\t" \
  "v_exp_f32 v160, v160\n\t" \
  "v_exp_f32 v161, v161\n\t" \
  "v_exp_f32 v162, v162\n\t" \
  "v_exp_f32 v163, v163\n\t" \
  "v_exp_f32 v164, v164\n\t" \
  "v_mfma_f32_32x32x16_bf16 v[176:191], v[4:7], v[208:211], v[128:143]\n\t" \
  "v_exp_f32 v165, v165\n\t" \
  "v_exp_f32 v166, v166\n\t" \
  "s_add_u32 m0, s62, 8192\n\t" \
  "v_exp_f32 v167, v167\n\t" \
  "buffer_load_dwordx4 v224, %[rs], s63 offen offset:0 lds\n\t" \
  "v_exp_f32 v168, v168\n\t" \
  "v_exp_f32 v169, v169\n\t" \
  "v_smfmac_f32_32x32x32_bf16 v[144:159], v[32:35], v[160:167], v222\n\t" \
  "v_exp_f32 v170, v170\n\t" \
  "v_exp_f32 v171, v171\n\t" \
  "v_and_b32 v225, 0x380, v224\n\t" \
  "v_add_u32 v225, v225, v224\n\t" \
  "v_exp_f32 v172, v172\n\t" \
  "v_exp_f32 v173, v173\n\t" \
  "v_exp_f32 v174, v174\n\t" \
  "v_exp_f32 v175, v175\n\t" \
  "v_exp_f32 v176, v176\n\t" \
  "v_exp_f32 v177, v177\n\t" \
  "v_smfmac_f32_32x32x32_bf16 v[144:159], v[36:39], v[168:175], v222\n\t" \
  "v_exp_f32 v178, v178\n\t" \
  "v_exp_f32 v179, v179\n\t" \
  "v_exp_f32 v180, v180\n\t" \
  "v_mfma_f32_32x32x16_bf16 v[160:175], v[8:11], v[208:211], v[128:143]\n\t" \
  "v_exp_f32 v181, v181\n\t" \
  "v_exp_f32 v182, v182\n\t" \
  "s_add_u32 m0, s62, 9216\n\t" \
  "v_exp_f32 v183, v183\n\t" \
  "buffer_load_dwordx4 v224, %[rs], s77 offen offset:0 lds\n\t" \
  "v_exp_f32 v184, v184\n\t" \
  "v_exp_f32 v185, v185\n\t" \
  "v_smfmac_f32_32x32x32_bf16 v[144:159], v[40:43], v[176:183], v222\n\t" \
  "v_exp_f32 v186, v186\n\t" \
  "v_exp_f32 v187, v187\n\t" \
  "v_exp_f32 v188, v188\n\t" \
  "v_exp_f32 v189, v189\n\t" \
  "v_exp_f32 v190, v190\n\t" \
  "v_exp_f32 v191, v191\n\t" \
  "v_exp_f32 v160, v160\n\t" \
  "v_exp_f32 v161, v161\n\t" \
  "v_smfmac_f32_32x32x32_bf16 v[144:159], v[44:47], v[184:191], v222\n\t" \
  "v_exp_f32 v162, v162\n\t" \
  "v_exp_f32 v163, v163\n\t" \
  "v_exp_f32 v164, v164\n\t" \
  "v_mfma_f32_32x32x16_bf16 v[176:191], v[12:15], v[208:211], v[128:143]\n\t" \
  "v_exp_f32 v165, v165\n\t" \
  "ds_read_b32 v223, v230 offset:40\n\t" \
  "v_exp_f32 v166, v166\n\t" \
  "v_exp_f32 v167, v167\n\t" \
  "v_exp_f32 v168, v168\n\t" \
  "v_exp_f32 v169, v169\n\t" \
  "v_smfmac_f32_32x32x32_bf16 v[144:159], v[48:51], v[160:167], v222\n\t" \
  "v_exp_f32 v170, v170\n\t" \
  "v_exp_f32 v171, v171\n\t" \
  "v_exp_f32 v172, v172\n\t" \
  "v_exp_f32 v173, v173\n\t" \
  "v_exp_f32 v174, v174\n\t" \
  "v_exp_f32 v175, v175\n\t" \
  "s_waitcnt lgkmcnt(0)\n\t" \
  "v_cmp_eq_u32 vcc, 0, v223\n\t" \
  "v_exp_f32 v176, v176\n\t" \
  "s_cbranch_vccnz LSPIN4%=\n\t" \
  "LBACK4%=:\n\t" \
  "ds_read_b128 v[212:215], v227 offset:2048\n\t" \
  "v_exp_f32 v177, v177\n\t" \
  "v_smfmac_f32_32x32x32_bf16 v[144:159], v[52:55], v[168:175], v222\n\t" \
  "v_exp_f32 v178, v178\n\t" \
  "v_exp_f32 v179, v179\n\t" \
  "v_exp_f32 v180, v180\n\t" \
  "v_mfma_f32_32x32x16_bf16 v[160:175], v[16:19], v[208:211], v[128:143]\n\t" \
  "v_exp_f32 v181, v181\n\t" \
  "v_exp_f32 v182, v182\n\t" \
  "v_exp_f32 v183, v183\n\t" \
  "v_exp_f32 v184, v184\n\t" \
  "v_exp_f32 v185, v185\n\t" \
  "v_smfmac_f32_32x32x32_bf16 v[144:159], v[56:59], v[176:183], v222\n\t" \
  "v_exp_f32 v186, v186\n\t" \
  "v_exp_f32 v187, v187\n\t" \
  "v_exp_f32 v188, v188\n\t" \
  "s_waitcnt vmcnt(3) lgkmcnt(0)\n\t" \
  "v_smfmac_f32_16x16x64_bf16 v[192:195], v[212:215], v[96:103], v229\n\t" \
  "v_exp_f32 v189, v189\n\t" \
  "v_exp_f32 v190, v190\n\t" \
  "v_exp_f32 v191, v191\n\t" \
  "v_exp_f32 v160, v160\n\t" \
  "v_exp_f32 v161, v161\n\t" \
  "v_smfmac_f32_32x32x32_bf16 v[144:159], v[60:63], v[184:191], v222\n\t" \
  "v_exp_f32 v162, v162\n\t" \
  "v_exp_f32 v163, v163\n\t" \
  "v_exp_f32 v164, v164\n\t" \
  "v_mfma_f32_32x32x16_bf16 v[176:191], v[20:23], v[208:211], v[128:143]\n\t" \
  "v_exp_f32 v165, v165\n\t" \
  "s_add_u32 s60, s60, 65536\n\t" \
  "v_exp_f32 v166, v166\n\t" \
  "buffer_load_dwordx4 v[96:99], v224, %[rs], s60 offen offset:0\n\t" \
  "v_exp_f32 v167, v167\n\t" \
  "v_exp_f32 v168, v168\n\t" \
  "buffer_load_dwordx4 v[100:103], v224, %[rs], s60 offen offset:1024\n\t" \
  "v_exp_f32 v169, v169\n\t" \
  "v_smfmac_f32_32x32x32_bf16 v[144:159], v[64:67], v[160:167], v222\n\t" \
  "v_exp_f32 v170, v170\n\t" \
  "v_exp_f32 v171, v171\n\t" \
  "v_exp_f32 v172, v172\n\t" \
  "v_smfmac_f32_16x16x64_bf16 v[196:199], v[212:215], v[104:111], v229\n\t" \
  "v_exp_f32 v173, v173\n\t" \
  "v_exp_f32 v174, v174\n\t" \
  "v_exp_f32 v175, v175\n\t" \
  "v_exp_f32 v176, v176\n\t" \
  "v_exp_f32 v177, v177\n\t" \
  "v_smfmac_f32_32x32x32_bf16 v[144:159], v[68:71], v[168:175], v222\n\t" \
  "v_exp_f32 v178, v178\n\t" \
  "v_exp_f32 v179, v179\n\t" \
  "v_exp_f32 v180, v180\n\t" \
  "v_mfma_f32_32x32x16_bf16 v[160:175], v[24:27], v[208:211], v[128:143]\n\t" \
  "v_exp_f32 v181, v181\n\t" \
  "v_exp_f32 v182, v182\n\t" \
  "buffer_load_dwordx4 v[104:107], v224, %[rs], s60 offen offset:2048\n\t" \
  "v_exp_f32 v183, v183\n\t" \
  "v_exp_f32 v184, v184\n\t" \
  "buffer_load_dwordx4 v[108:111], v224, %[rs], s60 offen offset:3072\n\t" \
  "v_exp_f32 v185, v185\n\t" \
  "v_smfmac_f32_32x32x32_bf16 v[144:159], v[72:75], v[176:183], v222\n\t" \
  "v_exp_f32 v186, v186\n\t" \
  "v_exp_f32 v187, v187\n\t" \
  "v_exp_f32 v188, v188\n\t" \
  "v_smfmac_f32_16x16x64_bf16 v[200:203], v[212:215], v[112:119], v229\n\t" \
  "v_exp_f32 v189, v189\n\t" \
  "v_exp_f32 v190, v190\n\t" \
  "v_exp_f32 v191, v191\n\t" \
  "v_exp_f32 v160, v160\n\t" \
  "v_exp_f32 v161, v161\n\t" \
  "v_smfmac_f32_32x32x32_bf16 v[144:159], v[76:79], v[184:191], v222\n\t" \
  "v_exp_f32 v162, v162\n\t" \
  "v_exp_f32 v163, v163\n\t" \
  "v_exp_f32 v164, v164\n\t" \
  "v_mfma_f32_32x32x16_bf16 v[176:191], v[28:31], v[208:211], v[128:143]\n\t" \
  "ds_read_b128 v[208:211], v226 offset:5120\n\t" \
  "v_exp_f32 v165, v165\n\t" \
  "s_add_u32 s61, s61, 65536\n\t" \
  "v_exp_f32 v166, v166\n\t" \
  "buffer_load_dwordx4 v[112:115], v224, %[rs], s61 offen offset:0\n\t" \
  "v_exp_f32 v167, v167\n\t" \
  "v_exp_f32 v168, v168\n\t" \
  "buffer_load_dwordx4 v[116:119], v224, %[rs], s61 offen offset:1024\n\t" \
  "v_exp_f32 v169, v169\n\t" \
  "v_smfmac_f32_32x32x32_bf16 v[144:159], v[80:83], v[160:167], v222\n\t" \
  "v_exp_f32 v170, v170\n\t" \
  "v_exp_f32 v171, v171\n\t" \
  "v_exp_f32 v172, v172\n\t" \
  "v_smfmac_f32_16x16x64_bf16 v[204:207], v[212:215], v[120:127], v229\n\t" \
  "v_exp_f32 v173, v173\n\t" \
  "v_exp_f32 v174, v174\n\t" \
  "v_exp_f32 v175, v175\n\t" \
  "v_exp_f32 v176, v176\n\t" \
  "v_exp_f32 v177, v177\n\t" \
  "v_smfmac_f32_32x32x32_bf16 v[144:159], v[84:87], v[168:175], v222\n\t" \
  "v_exp_f32 v178, v178\n\t" \
  "v_exp_f32 v179, v179\n\t" \
  "v_exp_f32 v180, v180\n\t" \
  "s_waitcnt lgkmcnt(0)\n\t" \
  "v_mfma_f32_32x32x16_bf16 v[160:175], v[0:3], v[208:211], v[128:143]\n\t" \
  "v_exp_f32 v181, v181\n\t" \
  "v_exp_f32 v182, v182\n\t" \
  "buffer_load_dwordx4 v[120:123], v224, %[rs], s61 offen offset:2048\n\t" \
  "v_exp_f32 v183, v183\n\t" \
  "v_exp_f32 v184, v184\n\t" \
  "buffer_load_dwordx4 v[124:127], v224, %[rs], s61 offen offset:3072\n\t" \
  "v_exp_f32 v185, v185\n\t" \
  "v_smfmac_f32_32x32x32_bf16 v[144:159], v[88:91], v[176:183], v222\n\t" \
  "v_exp_f32 v186, v186\n\t" \
  "v_exp_f32 v187, v187\n\t" \
  "buffer_store_dwordx4 v[252:255], v224, %[rs], %[sph] offen offset:2560 sc1\n\t" \
  "v_exp_f32 v188, v188\n\t" \
  "v_exp_f32 v189, v189\n\t" \
  "v_exp_f32 v190, v190\n\t" \
  "v_exp_f32 v191, v191\n\t" \
  "s_mov_b32 m0, s59\n\t" \
  "s_cmp_eq_u32 s72, 4\n\t" \
  "v_smfmac_f32_32x32x32_bf16 v[144:159], v[92:95], v[184:191], v222\n\t" \
  "s_nop 11\n\t" \
  "ds_write_b128 v228, v[144:147] offset:0\n\t" \
  "ds_write_b128 v228, v[148:151] offset:1024\n\t" \
  "ds_write_addtid_b32 v152 offset:2048\n\t" \
  "s_mov_b64 exec, 1\n\t" \
  "ds_sub_u32 v230, v252 offset:16\n\t" \
  "s_mov_b64 exec, -1\n\t" \
  "s_cbranch_scc1 LRED%=\n\t" \
  "LRB4%=:\n\t" \
  "s_setprio 1\n\t" \
  "v_mfma_f32_32x32x8_bf16 v[144:159], v[220:221], v[220:221], 0\n\t" \
  "v_exp_f32 v160, v160\n\t" \
  "v_exp_f32 v161, v161\n\t" \
  "v_exp_f32 v162, v162\n\t" \
  "v_exp_f32 v163, v163\n\t" \
  "v_exp_f32 v164, v164\n\t" \
  "v_mfma_f32_32x32x16_bf16 v[176:191], v[4:7], v[208:211], v[128:143]\n\t" \
  "v_exp_f32 v165, v165\n\t" \
  "v_exp_f32 v166, v166\n\t" \
  "s_add_u32 m0, s62, 9216\n\t" \
  "v_exp_f32 v167, v167\n\t" \
  "buffer_load_dwordx4 v224, %[rs], s63 offen offset:1024 lds\n\t" \
  "v_exp_f32 v168, v168\n\t" \
  "v_exp_f32 v169, v169\n\t" \
  "v_smfmac_f32_32x32x32_bf16 v[144:159], v[32:35], v[160:167], v222\n\t" \
  "v_exp_f32 v170, v170\n\t" \
  "v_exp_f32 v171, v171\n\t" \
  "v_exp_f32 v172, v172\n\t" \
  "v_exp_f32 v173, v173\n\t" \
  "v_exp_f32 v174, v174\n\t" \
  "v_exp_f32 v175, v175\n\t" \
  "v_exp_f32 v176, v176\n\t" \
  "v_exp_f32 v177, v177\n\t" \
  "v_smfmac_f32_32x32x32_bf16 v[144:159], v[36:39], v[168:175], v222\n\t" \
  "v_exp_f32 v178, v178\n\t" \
  "v_exp_f32 v179, v179\n\t" \
  "v_exp_f32 v180, v180\n\t" \
  "v_mfma_f32_32x32x16_bf16 v[160:175], v[8:11], v[208:211], v[128:143]\n\t" \
  "v_exp_f32 v181, v181\n\t" \
  "v_exp_f32 v182, v182\n\t" \
  "s_add_u32 m0, s62, 10240\n\t" \
  "v_exp_f32 v183, v183\n\t" \
  "buffer_load_dwordx4 v224, %[rs], s77 offen offset:1024 lds\n\t" \
  "v_exp_f32 v184, v184\n\t" \
  "v_exp_f32 v185, v185\n\t" \
  "v_smfmac_f32_32x32x32_bf16 v[144:159], v[40:43], v[176:183], v222\n\t" \
  "v_exp_f32 v186, v186\n\t" \
  "v_exp_f32 v187, v187\n\t" \
  "v_exp_f32 v188, v188\n\t" \
  "v_exp_f32 v189, v189\n\t" \
  "v_exp_f32 v190, v190\n\t" \
  "v_exp_f32 v191, v191\n\t" \
  "v_exp_f32 v160, v160\n\t" \
  "v_exp_f32 v161, v161\n\t" \
  "v_smfmac_f32_32x32x32_bf16 v[144:159], v[44:47], v[184:191], v222\n\t" \
  "v_exp_f32 v162, v162\n\t" \
  "v_exp_f32 v163, v163\n\t" \
  "v_exp_f32 v164, v164\n\t" \
  "v_mfma_f32_32x32x16_bf16 v[176:191], v[12:15], v[208:211], v[128:143]\n\t" \
  "v_exp_f32 v165, v165\n\t" \
  "ds_read_b32 v223, v230 offset:44\n\t" \
  "v_exp_f32 v166, v166\n\t" \
  "v_exp_f32 v167, v167\n\t" \
  "v_exp_f32 v168, v168\n\t" \
  "v_exp_f32 v169, v169\n\t" \
  "v_smfmac_f32_32x32x32_bf16 v[144:159], v[48:51], v[160:167], v222\n\t" \
  "v_exp_f32 v170, v170\n\t" \
  "v_exp_f32 v171, v171\n\t" \
  "v_exp_f32 v172, v172\n\t" \
  "v_exp_f32 v173, v173\n\t" \
  "v_exp_f32 v174, v174\n\t" \
  "v_exp_f32 v175, v175\n\t" \
  "s_waitcnt lgkmcnt(0)\n\t" \
  "v_cmp_eq_u32 vcc, 0, v223\n\t" \
  "v_exp_f32 v176, v176\n\t" \
  "s_cbranch_vccnz LSPIN5%=\n\t" \
  "LBACK5%=:\n\t" \
  "ds_read_b128 v[212:215], v227 offset:3072\n\t" \
  "v_exp_f32 v177, v177\n\t" \
  "v_smfmac_f32_32x32x32_bf16 v[144:159], v[52:55], v[168:175], v222\n\t" \
  "v_exp_f32 v178, v178\n\t" \
  "v_exp_f32 v179, v179\n\t" \
  "v_exp_f32 v180, v180\n\t" \
  "v_mfma_f32_32x32x16_bf16 v[160:175], v[16:19], v[208:211], v[128:143]\n\t" \
  "v_exp_f32 v181, v181\n\t" \
  "v_exp_f32 v182, v182\n\t" \
  "v_exp_f32 v183, v183\n\t" \
  "v_exp_f32 v184, v184\n\t" \
  "v_exp_f32 v185, v185\n\t" \
  "v_smfmac_f32_32x32x32_bf16 v[144:159], v[56:59], v[176:183], v222\n\t" \
  "v_exp_f32 v186, v186\n\t" \
  "v_exp_f32 v187, v187\n\t" \
  "v_exp_f32 v188, v188\n\t" \
  "s_waitcnt vmcnt(3) lgkmcnt(0)\n\t" \
  "v_smfmac_f32_16x16x64_bf16 v[192:195], v[212:215], v[96:103], v229\n\t" \
  "v_exp_f32 v189, v189\n\t" \
  "v_exp_f32 v190, v190\n\t" \
  "v_exp_f32 v191, v191\n\t" \
  "v_exp_f32 v160, v160\n\t" \
  "v_exp_f32 v161, v161\n\t" \
  "v_smfmac_f32_32x32x32_bf16 v[144:159], v[60:63], v[184:191], v222\n\t" \
  "v_exp_f32 v162, v162\n\t" \
  "v_exp_f32 v163, v163\n\t" \
  "v_exp_f32 v164, v164\n\t" \
  "v_mfma_f32_32x32x16_bf16 v[176:191], v[20:23], v[208:211], v[128:143]\n\t" \
  "v_exp_f32 v165, v165\n\t" \
  "s_add_u32 s60, s60, 65536\n\t" \
  "v_exp_f32 v166, v166\n\t" \
  "buffer_load_dwordx4 v[96:99], v224, %[rs], s60 offen offset:0\n\t" \
  "v_exp_f32 v167, v167\n\t" \
  "v_exp_f32 v168, v168\n\t" \
  "buffer_load_dwordx4 v[100:103], v224, %[rs], s60 offen offset:1024\n\t" \
  "v_exp_f32 v169, v169\n\t" \
  "v_smfmac_f32_32x32x32_bf16 v[144:159], v[64:67], v[160:167], v222\n\t" \
  "v_exp_f32 v170, v170\n\t" \
  "v_exp_f32 v171, v171\n\t" \
  "v_exp_f32 v172, v172\n\t" \
  "v_smfmac_f32_16x16x64_bf16 v[196:199], v[212:215], v[104:111], v229\n\t" \
  "v_exp_f32 v173, v173\n\t" \
  "v_exp_f32 v174, v174\n\t" \
  "v_exp_f32 v175, v175\n\t" \
  "v_exp_f32 v176, v176\n\t" \
  "v_exp_f32 v177, v177\n\t" \
  "v_smfmac_f32_32x32x32_bf16 v[144:159], v[68:71], v[168:175], v222\n\t" \
  "v_exp_f32 v178, v178\n\t" \
  "v_exp_f32 v179, v179\n\t" \
  "v_exp_f32 v180, v180\n\t" \
  "v_mfma_f32_32x32x16_bf16 v[160:175], v[24:27], v[208:211], v[128:143]\n\t" \
  "v_exp_f32 v181, v181\n\t" \
  "v_exp_f32 v182, v182\n\t" \
  "buffer_load_dwordx4 v[104:107], v224, %[rs], s60 offen offset:2048\n\t" \
  "v_exp_f32 v183, v183\n\t" \
  "v_exp_f32 v184, v184\n\t" \
  "buffer_load_dwordx4 v[108:111], v224, %[rs], s60 offen offset:3072\n\t" \
  "v_exp_f32 v185, v185\n\t" \
  "v_smfmac_f32_32x32x32_bf16 v[144:159], v[72:75], v[176:183], v222\n\t" \
  "v_exp_f32 v186, v186\n\t" \
  "v_exp_f32 v187, v187\n\t" \
  "v_exp_f32 v188, v188\n\t" \
  "v_smfmac_f32_16x16x64_bf16 v[200:203], v[212:215], v[112:119], v229\n\t" \
  "v_exp_f32 v189, v189\n\t" \
  "v_exp_f32 v190, v190\n\t" \
  "v_exp_f32 v191, v191\n\t" \
  "v_exp_f32 v160, v160\n\t" \
  "v_exp_f32 v161, v161\n\t" \
  "v_smfmac_f32_32x32x32_bf16 v[144:159], v[76:79], v[184:191], v222\n\t" \
  "v_exp_f32 v162, v162\n\t" \
  "v_exp_f32 v163, v163\n\t" \
  "v_exp_f32 v164, v164\n\t" \
  "v_mfma_f32_32x32x16_bf16 v[176:191], v[28:31], v[208:211], v[128:143]\n\t" \
  "ds_read_b128 v[208:211], v226 offset:6144\n\t" \
  "v_exp_f32 v165, v165\n\t" \
  "s_add_u32 s61, s61, 65536\n\t" \
  "v_exp_f32 v166, v166\n\t" \
  "buffer_load_dwordx4 v[112:115], v224, %[rs], s61 offen offset:0\n\t" \
  "v_exp_f32 v167, v167\n\t" \
  "v_exp_f32 v168, v168\n\t" \
  "buffer_load_dwordx4 v[116:119], v224, %[rs], s61 offen offset:1024\n\t" \
  "v_exp_f32 v169, v169\n\t" \
  "v_smfmac_f32_32x32x32_bf16 v[144:159], v[80:83], v[160:167], v222\n\t" \
  "v_exp_f32 v170, v170\n\t" \
  "v_exp_f32 v171, v171\n\t" \
  "v_exp_f32 v172, v172\n\t" \
  "v_smfmac_f32_16x16x64_bf16 v[204:207], v[212:215], v[120:127], v229\n\t" \
  "v_exp_f32 v173, v173\n\t" \
  "v_exp_f32 v174, v174\n\t" \
  "v_exp_f32 v175, v175\n\t" \
  "v_exp_f32 v176, v176\n\t" \
  "v_exp_f32 v177, v177\n\t" \
  "v_smfmac_f32_32x32x32_bf16 v[144:159], v[84:87], v[168:175], v222\n\t" \
  "v_exp_f32 v178, v178\n\t" \
  "v_exp_f32 v179, v179\n\t" \
  "v_exp_f32 v180, v180\n\t" \
  "s_waitcnt lgkmcnt(0)\n\t" \
  "v_mfma_f32_32x32x16_bf16 v[160:175], v[0:3], v[208:211], v[128:143]\n\t" \
  "v_exp_f32 v181, v181\n\t" \
  "v_exp_f32 v182, v182\n\t" \
  "buffer_load_dwordx4 v[120:123], v224, %[rs], s61 offen offset:2048\n\t" \
  "v_exp_f32 v183, v183\n\t" \
  "v_exp_f32 v184, v184\n\t" \
  "buffer_load_dwordx4 v[124:127], v224, %[rs], s61 offen offset:3072\n\t" \
  "v_exp_f32 v185, v185\n\t" \
  "v_smfmac_f32_32x32x32_bf16 v[144:159], v[88:91], v[176:183], v222\n\t" \
  "v_exp_f32 v186, v186\n\t" \
  "v_exp_f32 v187, v187\n\t" \
  "buffer_store_dwordx4 v[252:255], v231, %[rs], %[spx] offen sc1\n\t" \
  "v_exp_f32 v188, v188\n\t" \
  "v_exp_f32 v189, v189\n\t" \
  "v_exp_f32 v190, v190\n\t" \
  "v_exp_f32 v191, v191\n\t" \
  "s_mov_b32 m0, s59\n\t" \
  "s_cmp_eq_u32 s72, 5\n\t" \
  "v_smfmac_f32_32x32x32_bf16 v[144:159], v[92:95], v[184:191], v222\n\t" \
  "s_nop 11\n\t" \
  "ds_write_b128 v228, v[144:147] offset:18432\n\t" \
  "ds_write_b128 v228, v[148:151] offset:19456\n\t" \
  "ds_write_addtid_b32 v152 offset:20480\n\t" \
  "s_mov_b64 exec, 1\n\t" \
  "ds_sub_u32 v230, v252 offset:20\n\t" \
  "s_mov_b64 exec, -1\n\t" \
  "s_cbranch_scc1 LRED%=\n\t" \
  "LRB5%=:\n\t" \
  "s_cmp_lt_u32 %[w], 4\n\t" \
  "s_cbranch_scc0 LP6%=\n\t" \
  "s_setprio 0\n\t" \
  "LP6%=:\n\t" \
  "v_mfma_f32_32x32x8_bf16 v[144:159], v[220:221], v[220:221], 0\n\t" \
  "v_exp_f32 v160, v160\n\t" \
  "v_exp_f32 v161, v161\n\t" \
  "v_exp_f32 v162, v162\n\t" \
  "v_exp_f32 v163, v163\n\t" \
  "v_exp_f32 v164, v164\n\t" \
  "v_mfma_f32_32x32x16_bf16 v[176:191], v[4:7], v[208:211], v[128:143]\n\t" \
  "v_exp_f32 v165, v165\n\t" \
  "v_exp_f32 v166, v166\n\t" \
  "s_add_u32 m0, s62, 10240\n\t" \
  "v_exp_f32 v167, v167\n\t" \
  "buffer_load_dwordx4 v224, %[rs], s63 offen offset:2048 lds\n\t" \
  "v_exp_f32 v168, v168\n\t" \
  "v_exp_f32 v169, v169\n\t" \
  "v_smfmac_f32_32x32x32_bf16 v[144:159], v[32:35], v[160:167], v222\n\t" \
  "v_exp_f32 v170, v170\n\t" \
  "v_exp_f32 v171, v171\n\t" \
  "v_exp_f32 v172, v172\n\t" \
  "v_exp_f32 v173, v173\n\t" \
  "v_exp_f32 v174, v174\n\t" \
  "v_exp_f32 v175, v175\n\t" \
  "v_exp_f32 v176, v176\n\t" \
  "v_exp_f32 v177, v177\n\t" \
  "v_smfmac_f32_32x32x32_bf16 v[144:159], v[36:39], v[168:175], v222\n\t" \
  "v_exp_f32 v178, v178\n\t" \
  "v_exp_f32 v179, v179\n\t" \
  "v_exp_f32 v180, v180\n\t" \
  "v_mfma_f32_32x32x16_bf16 v[160:175], v[8:11], v[208:211], v[128:143]\n\t" \
  "v_exp_f32 v181, v181\n\t" \
  "v_exp_f32 v182, v182\n\t" \
  "s_add_u32 m0, s62, 11264\n\t" \
  "v_exp_f32 v183, v183\n\t" \
  "buffer_load_dwordx4 v224, %[rs], s77 offen offset:2048 lds\n\t" \
  "v_exp_f32 v184, v184\n\t" \
  "v_exp_f32 v185, v185\n\t" \
  "v_smfmac_f32_32x32x32_bf16 v[144:159], v[40:43], v[176:183], v222\n\t" \
  "v_exp_f32 v186, v186\n\t" \
  "v_exp_f32 v187, v187\n\t" \
  "v_exp_f32 v188, v188\n\t" \
  "v_exp_f32 v189, v189\n\t" \
  "v_exp_f32 v190, v190\n\t" \
  "v_exp_f32 v191, v191\n\t" \
  "v_exp_f32 v160, v160\n\t" \
  "v_exp_f32 v161, v161\n\t" \
  "v_smfmac_f32_32x32x32_bf16 v[144:159], v[44:47], v[184:191], v222\n\t" \
  "v_exp_f32 v162, v162\n\t" \
  "v_exp_f32 v163, v163\n\t" \
  "v_exp_f32 v164, v164\n\t" \
  "v_mfma_f32_32x32x16_bf16 v[176:191], v[12:15], v[208:211], v[128:143]\n\t" \
  "v_exp_f32 v165, v165\n\t" \
  "ds_read_b32 v223, v230 offset:48\n\t" \
  "v_exp_f32 v166, v166\n\t" \
  "v_exp_f32 v167, v167\n\t" \
  "v_exp_f32 v168, v168\n\t" \
  "v_exp_f32 v169, v169\n\t" \
  "v_smfmac_f32_32x32x32_bf16 v[144:159], v[48:51], v[160:167], v222\n\t" \
  "v_exp_f32 v170, v170\n\t" \
  "v_exp_f32 v171, v171\n\t" \
  "v_exp_f32 v172, v172\n\t" \
  "v_exp_f32 v173, v173\n\t" \
  "v_exp_f32 v174, v174\n\t" \
  "v_exp_f32 v175, v175\n\t" \
  "s_waitcnt lgkmcnt(0)\n\t" \
  "v_cmp_eq_u32 vcc, 0, v223\n\t" \
  "v_exp_f32 v176, v176\n\t" \
  "s_cbranch_vccnz LSPIN6%=\n\t" \
  "LBACK6%=:\n\t" \
  "ds_read_b128 v[212:215], v227 offset:4096\n\t" \
  "v_exp_f32 v177, v177\n\t" \
  "v_smfmac_f32_32x32x32_bf16 v[144:159], v[52:55], v[168:175], v222\n\t" \
  "v_exp_f32 v178, v178\n\t" \
  "v_exp_f32 v179, v179\n\t" \
  "v_exp_f32 v180, v180\n\t" \
  "v_mfma_f32_32x32x16_bf16 v[160:175], v[16:19], v[208:211], v[128:143]\n\t" \
  "v_exp_f32 v181, v181\n\t" \
  "v_exp_f32 v182, v182\n\t" \
  "v_exp_f32 v183, v183\n\t" \
  "v_exp_f32 v184, v184\n\t" \
  "v_exp_f32 v185, v185\n\t" \
  "v_smfmac_f32_32x32x32_bf16 v[144:159], v[56:59], v[176:183], v222\n\t" \
  "v_exp_f32 v186, v186\n\t" \
  "v_exp_f32 v187, v187\n\t" \
  "v_exp_f32 v188, v188\n\t" \
  "s_waitcnt vmcnt(3) lgkmcnt(0)\n\t" \
  "v_smfmac_f32_16x16x64_bf16 v[192:195], v[212:215], v[96:103], v229\n\t" \
  "v_exp_f32 v189, v189\n\t" \
  "v_exp_f32 v190, v190\n\t" \
  "v_exp_f32 v191, v191\n\t" \
  "v_exp_f32 v160, v160\n\t" \
  "v_exp_f32 v161, v161\n\t" \
  "v_smfmac_f32_32x32x32_bf16 v[144:159], v[60:63], v[184:191], v222\n\t" \
  "v_exp_f32 v162, v162\n\t" \
  "v_exp_f32 v163, v163\n\t" \
  "v_exp_f32 v164, v164\n\t" \
  "v_mfma_f32_32x32x16_bf16 v[176:191], v[20:23], v[208:211], v[128:143]\n\t" \
  "v_exp_f32 v165, v165\n\t" \
  "s_add_u32 s60, s60, 65536\n\t" \
  "v_exp_f32 v166, v166\n\t" \
  "buffer_load_dwordx4 v[96:99], v224, %[rs], s60 offen offset:0\n\t" \
  "v_exp_f32 v167, v167\n\t" \
  "v_exp_f32 v168, v168\n\t" \
  "buffer_load_dwordx4 v[100:103], v224, %[rs], s60 offen offset:1024\n\t" \
  "v_exp_f32 v169, v169\n\t" \
  "v_smfmac_f32_32x32x32_bf16 v[144:159], v[64:67], v[160:167], v222\n\t" \
  "v_exp_f32 v170, v170\n\t" \
  "v_exp_f32 v171, v171\n\t" \
  "v_exp_f32 v172, v172\n\t" \
  "v_smfmac_f32_16x16x64_bf16 v[196:199], v[212:215], v[104:111], v229\n\t" \
  "v_exp_f32 v173, v173\n\t" \
  "v_exp_f32 v174, v174\n\t" \
  "v_exp_f32 v175, v175\n\t" \
  "v_exp_f32 v176, v176\n\t" \
  "v_exp_f32 v177, v177\n\t" \
  "v_smfmac_f32_32x32x32_bf16 v[144:159], v[68:71], v[168:175], v222\n\t" \
  "v_exp_f32 v178, v178\n\t" \
  "v_exp_f32 v179, v179\n\t" \
  "v_exp_f32 v180, v180\n\t" \
  "v_mfma_f32_32x32x16_bf16 v[160:175], v[24:27], v[208:211], v[128:143]\n\t" \
  "v_exp_f32 v181, v181\n\t" \
  "v_exp_f32 v182, v182\n\t" \
  "buffer_load_dwordx4 v[104:107], v224, %[rs], s60 offen offset:2048\n\t" \
  "v_exp_f32 v183, v183\n\t" \
  "v_exp_f32 v184, v184\n\t" \
  "buffer_load_dwordx4 v[108:111], v224, %[rs], s60 offen offset:3072\n\t" \
  "v_exp_f32 v185, v185\n\t" \
  "v_smfmac_f32_32x32x32_bf16 v[144:159], v[72:75], v[176:183], v222\n\t" \
  "v_exp_f32 v186, v186\n\t" \
  "v_exp_f32 v187, v187\n\t" \
  "v_exp_f32 v188, v188\n\t" \
  "v_smfmac_f32_16x16x64_bf16 v[200:203], v[212:215], v[112:119], v229\n\t" \
  "v_exp_f32 v189, v189\n\t" \
  "v_exp_f32 v190, v190\n\t" \
  "v_exp_f32 v191, v191\n\t" \
  "v_exp_f32 v160, v160\n\t" \
  "v_exp_f32 v161, v161\n\t" \
  "v_smfmac_f32_32x32x32_bf16 v[144:159], v[76:79], v[184:191], v222\n\t" \
  "v_exp_f32 v162, v162\n\t" \
  "v_exp_f32 v163, v163\n\t" \
  "v_exp_f32 v164, v164\n\t" \
  "v_mfma_f32_32x32x16_bf16 v[176:191], v[28:31], v[208:211], v[128:143]\n\t" \
  "ds_read_b128 v[208:211], v226 offset:7168\n\t" \
  "v_exp_f32 v165, v165\n\t" \
  "s_add_u32 s61, s61, 65536\n\t" \
  "v_exp_f32 v166, v166\n\t" \
  "buffer_load_dwordx4 v[112:115], v224, %[rs], s61 offen offset:0\n\t" \
  "v_exp_f32 v167, v167\n\t" \
  "v_exp_f32 v168, v168\n\t" \
  "buffer_load_dwordx4 v[116:119], v224, %[rs], s61 offen offset:1024\n\t" \
  "v_exp_f32 v169, v169\n\t" \
  "v_smfmac_f32_32x32x32_bf16 v[144:159], v[80:83], v[160:167], v222\n\t" \
  "v_exp_f32 v170, v170\n\t" \
  "v_exp_f32 v171, v171\n\t" \
  "v_exp_f32 v172, v172\n\t" \
  "v_smfmac_f32_16x16x64_bf16 v[204:207], v[212:215], v[120:127], v229\n\t" \
  "v_exp_f32 v173, v173\n\t" \
  "v_exp_f32 v174, v174\n\t" \
  "v_exp_f32 v175, v175\n\t" \
  "v_exp_f32 v176, v176\n\t" \
  "v_exp_f32 v177, v177\n\t" \
  "v_smfmac_f32_32x32x32_bf16 v[144:159], v[84:87], v[168:175], v222\n\t" \
  "v_exp_f32 v178, v178\n\t" \
  "v_exp_f32 v179, v179\n\t" \
  "v_exp_f32 v180, v180\n\t" \
  "s_waitcnt lgkmcnt(0)\n\t" \
  "v_mfma_f32_32x32x16_bf16 v[160:175], v[0:3], v[208:211], v[128:143]\n\t" \
  "v_exp_f32 v181, v181\n\t" \
  "v_exp_f32 v182, v182\n\t" \
  "buffer_load_dwordx4 v[120:123], v224, %[rs], s61 offen offset:2048\n\t" \
  "v_exp_f32 v183, v183\n\t" \
  "v_exp_f32 v184, v184\n\t" \
  "buffer_load_dwordx4 v[124:127], v224, %[rs], s61 offen offset:3072\n\t" \
  "v_exp_f32 v185, v185\n\t" \
  "v_smfmac_f32_32x32x32_bf16 v[144:159], v[88:91], v[176:183], v222\n\t" \
  "v_exp_f32 v186, v186\n\t" \
  "v_exp_f32 v187, v187\n\t" \
  "buffer_store_dwordx4 v[252:255], v225, %[rs], %[spn] offen sc1\n\t" \
  "v_exp_f32 v188, v188\n\t" \
  "v_exp_f32 v189, v189\n\t" \
  "v_exp_f32 v190, v190\n\t" \
  "v_exp_f32 v191, v191\n\t" \
  "s_mov_b32 m0, s59\n\t" \
  "s_cmp_eq_u32 s72, 6\n\t" \
  "v_smfmac_f32_32x32x32_bf16 v[144:159], v[92:95], v[184:191], v222\n\t" \
  "s_nop 11\n\t" \
  "ds_write_b128 v228, v[144:147] offset:0\n\t" \
  "ds_write_b128 v228, v[148:151] offset:1024\n\t" \
  "ds_write_addtid_b32 v152 offset:2048\n\t" \
  "s_mov_b64 exec, 1\n\t" \
  "ds_sub_u32 v230, v252 offset:24\n\t" \
  "s_mov_b64 exec, -1\n\t" \
  "s_cbranch_scc1 LRED%=\n\t" \
  "LRB6%=:\n\t" \
  "v_mfma_f32_32x32x8_bf16 v[144:159], v[220:221], v[220:221], 0\n\t" \
  "v_exp_f32 v160, v160\n\t" \
  "s_add_u32 s76, s60, 131072\n\t" \
  "s_add_u32 s77, s61, 131072\n\t" \
  "v_exp_f32 v161, v161\n\t" \
  "buffer_load_dwordx4 v[0:3], v224, %[rs], s76 offen offset:0\n\t" \
  "v_exp_f32 v162, v162\n\t" \
  "v_exp_f32 v163, v163\n\t" \
  "v_exp_f32 v164, v164\n\t" \
  "v_mfma_f32_32x32x16_bf16 v[176:191], v[4:7], v[208:211], v[128:143]\n\t" \
  "v_exp_f32 v165, v165\n\t" \
  "v_exp_f32 v166, v166\n\t" \
  "v_exp_f32 v167, v167\n\t" \
  "v_exp_f32 v168, v168\n\t" \
  "v_exp_f32 v169, v169\n\t" \
  "v_smfmac_f32_32x32x32_bf16 v[144:159], v[32:35], v[160:167], v222\n\t" \
  "v_exp_f32 v170, v170\n\t" \
  "v_exp_f32 v171, v171\n\t" \
  "v_exp_f32 v172, v172\n\t" \
  "v_exp_f32 v173, v173\n\t" \
  "v_exp_f32 v174, v174\n\t" \
  "v_exp_f32 v175, v175\n\t" \
  "v_exp_f32 v176, v176\n\t" \
  "v_exp_f32 v177, v177\n\t" \
  "v_smfmac_f32_32x32x32_bf16 v[144:159], v[36:39], v[168:175], v222\n\t" \
  "v_exp_f32 v178, v178\n\t" \
  "buffer_load_dwordx4 v[4:7], v224, %[rs], s76 offen offset:1024\n\t" \
  "v_exp_f32 v179, v179\n\t" \
  "v_exp_f32 v180, v180\n\t" \
  "v_mfma_f32_32x32x16_bf16 v[160:175], v[8:11], v[208:211], v[128:143]\n\t" \
  "v_exp_f32 v181, v181\n\t" \
  "v_exp_f32 v182, v182\n\t" \
  "v_exp_f32 v183, v183\n\t" \
  "v_exp_f32 v184, v184\n\t" \
  "v_exp_f32 v185, v185\n\t" \
  "v_smfmac_f32_32x32x32_bf16 v[144:159], v[40:43], v[176:183], v222\n\t" \
  "v_exp_f32 v186, v186\n\t" \
  "v_exp_f32 v187, v187\n\t" \
  "v_exp_f32 v188, v188\n\t" \
  "v_exp_f32 v189, v189\n\t" \
  "v_exp_f32 v190, v190\n\t" \
  "v_exp_f32 v191, v191\n\t" \
  "v_exp_f32 v160, v160\n\t" \
  "v_exp_f32 v161, v161\n\t" \
  "v_smfmac_f32_32x32x32_bf16 v[144:159], v[44:47], v[184:191], v222\n\t" \
  "v_exp_f32 v162, v162\n\t" \
  "buffer_load_dwordx4 v[8:11], v224, %[rs], s76 offen offset:2048\n\t" \
  "v_exp_f32 v163, v163\n\t" \
  "s_setprio 0\n\t" \
  "v_exp_f32 v164, v164\n\t" \
  "v_mfma_f32_32x32x16_bf16 v[176:191], v[12:15], v[208:211], v[128:143]\n\t" \
  "v_exp_f32 v165, v165\n\t" \
  "ds_read_b32 v223, v230 offset:52\n\t" \
  "v_exp_f32 v166, v166\n\t" \
  "v_exp_f32 v167, v167\n\t" \
  "v_exp_f32 v168, v168\n\t" \
  "v_exp_f32 v169, v169\n\t" \
  "v_smfmac_f32_32x32x32_bf16 v[144:159], v[48:51], v[160:167], v222\n\t" \
  "v_exp_f32 v170, v170\n\t" \
  "v_exp_f32 v171, v171\n\t" \
  "v_exp_f32 v172, v172\n\t" \
  "v_exp_f32 v173, v173\n\t" \
  "v_exp_f32 v174, v174\n\t" \
  "v_exp_f32 v175, v175\n\t" \
  "s_waitcnt lgkmcnt(0)\n\t" \
  "v_cmp_eq_u32 vcc, 0, v223\n\t" \
  "v_exp_f32 v176, v176\n\t" \
  "s_cbranch_vccnz LSPIN7%=\n\t" \
  "LBACK7%=:\n\t" \
  "ds_read_b128 v[212:215], v227 offset:5120\n\t" \
  "v_exp_f32 v177, v177\n\t" \
  "v_smfmac_f32_32x32x32_bf16 v[144:159], v[52:55], v[168:175], v222\n\t" \
  "v_exp_f32 v178, v178\n\t" \
  "buffer_load_dwordx4 v[12:15], v224, %[rs], s76 offen offset:3072\n\t" \
  "v_exp_f32 v179, v179\n\t" \
  "v_exp_f32 v180, v180\n\t" \
  "v_mfma_f32_32x32x16_bf16 v[160:175], v[16:19], v[208:211], v[128:143]\n\t" \
  "v_exp_f32 v181, v181\n\t" \
  "v_exp_f32 v182, v182\n\t" \
  "v_exp_f32 v183, v183\n\t" \
  "v_exp_f32 v184, v184\n\t" \
  "v_exp_f32 v185, v185\n\t" \
  "v_smfmac_f32_32x32x32_bf16 v[144:159], v[56:59], v[176:183], v222\n\t" \
  "v_exp_f32 v186, v186\n\t" \
  "v_exp_f32 v187, v187\n\t" \
  "v_exp_f32 v188, v188\n\t" \
  "s_waitcnt vmcnt(5) lgkmcnt(0)\n\t" \
  "v_smfmac_f32_16x16x64_bf16 v[192:195], v[212:215], v[96:103], v229\n\t" \
  "v_exp_f32 v189, v189\n\t" \
  "v_exp_f32 v190, v190\n\t" \
  "v_exp_f32 v191, v191\n\t" \
  "v_exp_f32 v160, v160\n\t" \
  "v_exp_f32 v161, v161\n\t" \
  "v_smfmac_f32_32x32x32_bf16 v[144:159], v[60:63], v[184:191], v222\n\t" \
  "v_exp_f32 v162, v162\n\t" \
  "buffer_load_dwordx4 v[16:19], v224, %[rs], s77 offen offset:0\n\t" \
  "v_exp_f32 v163, v163\n\t" \
  "v_exp_f32 v164, v164\n\t" \
  "v_mfma_f32_32x32x16_bf16 v[176:191], v[20:23], v[208:211], v[128:143]\n\t" \
  "v_exp_f32 v165, v165\n\t" \
  "s_add_u32 s60, s60, 65536\n\t" \
  "v_exp_f32 v166, v166\n\t" \
  "buffer_load_dwordx4 v[96:99], v224, %[rs], s60 offen offset:0\n\t" \
  "v_exp_f32 v167, v167\n\t" \
  "v_exp_f32 v168, v168\n\t" \
  "buffer_load_dwordx4 v[100:103], v224, %[rs], s60 offen offset:1024\n\t" \
  "v_exp_f32 v169, v169\n\t" \
  "v_smfmac_f32_32x32x32_bf16 v[144:159], v[64:67], v[160:167], v222\n\t" \
  "v_exp_f32 v170, v170\n\t" \
  "v_exp_f32 v171, v171\n\t" \
  "v_exp_f32 v172, v172\n\t" \
  "v_smfmac_f32_16x16x64_bf16 v[196:199], v[212:215], v[104:111], v229\n\t" \
  "v_exp_f32 v173, v173\n\t" \
  "v_exp_f32 v174, v174\n\t" \
  "v_exp_f32 v175, v175\n\t" \
  "v_exp_f32 v176, v176\n\t" \
  "v_exp_f32 v177, v177\n\t" \
  "v_smfmac_f32_32x32x32_bf16 v[144:159], v[68:71], v[168:175], v222\n\t" \
  "v_exp_f32 v178, v178\n\t" \
  "buffer_load_dwordx4 v[20:23], v224, %[rs], s77 offen offset:1024\n\t" \
  "v_exp_f32 v179, v179\n\t" \
  "v_exp_f32 v180, v180\n\t" \
  "v_mfma_f32_32x32x16_bf16 v[160:175], v[24:27], v[208:211], v[128:143]\n\t" \
  "v_exp_f32 v181, v181\n\t" \
  "v_exp_f32 v182, v182\n\t" \
  "buffer_load_dwordx4 v[104:107], v224, %[rs], s60 offen offset:2048\n\t" \
  "v_exp_f32 v183, v183\n\t" \
  "v_exp_f32 v184, v184\n\t" \
  "buffer_load_dwordx4 v[108:111], v224, %[rs], s60 offen offset:3072\n\t" \
  "v_exp_f32 v185, v185\n\t" \
  "v_smfmac_f32_32x32x32_bf16 v[144:159], v[72:75], v[176:183], v222\n\t" \
  "v_exp_f32 v186, v186\n\t" \
  "v_exp_f32 v187, v187\n\t" \
  "v_exp_f32 v188, v188\n\t" \
  "v_smfmac_f32_16x16x64_bf16 v[200:203], v[212:215], v[112:119], v229\n\t" \
  "v_exp_f32 v189, v189\n\t" \
  "v_exp_f32 v190, v190\n\t" \
  "v_exp_f32 v191, v191\n\t" \
  "v_exp_f32 v160, v160\n\t" \
  "v_exp_f32 v161, v161\n\t" \
  "v_smfmac_f32_32x32x32_bf16 v[144:159], v[76:79], v[184:191], v222\n\t" \
  "v_exp_f32 v162, v162\n\t" \
  "buffer_load_dwordx4 v[24:27], v224, %[rs], s77 offen offset:2048\n\t" \
  "v_exp_f32 v163, v163\n\t" \
  "v_exp_f32 v164, v164\n\t" \
  "v_mfma_f32_32x32x16_bf16 v[176:191], v[28:31], v[208:211], v[128:143]\n\t" \
  "v_exp_f32 v165, v165\n\t" \
  "s_add_u32 s61, s61, 65536\n\t" \
  "v_exp_f32 v166, v166\n\t" \
  "buffer_load_dwordx4 v[112:115], v224, %[rs], s61 offen offset:0\n\t" \
  "v_exp_f32 v167, v167\n\t" \
  "v_exp_f32 v168, v168\n\t" \
  "buffer_load_dwordx4 v[116:119], v224, %[rs], s61 offen offset:1024\n\t" \
  "v_exp_f32 v169, v169\n\t" \
  "v_smfmac_f32_32x32x32_bf16 v[144:159], v[80:83], v[160:167], v222\n\t" \
  "v_exp_f32 v170, v170\n\t" \
  "v_exp_f32 v171, v171\n\t" \
  "v_exp_f32 v172, v172\n\t" \
  "v_smfmac_f32_16x16x64_bf16 v[204:207], v[212:215], v[120:127], v229\n\t" \
  "v_exp_f32 v173, v173\n\t" \
  "v_exp_f32 v174, v174\n\t" \
  "v_exp_f32 v175, v175\n\t" \
  "v_exp_f32 v176, v176\n\t" \
  "v_exp_f32 v177, v177\n\t" \
  "v_smfmac_f32_32x32x32_bf16 v[144:159], v[84:87], v[168:175], v222\n\t" \
  "v_exp_f32 v178, v178\n\t" \
  "buffer_load_dwordx4 v[28:31], v224, %[rs], s77 offen offset:3072\n\t" \
  "v_exp_f32 v179, v179\n\t" \
  "v_exp_f32 v180, v180\n\t" \
  "v_exp_f32 v181, v181\n\t" \
  "v_exp_f32 v182, v182\n\t" \
  "buffer_load_dwordx4 v[120:123], v224, %[rs], s61 offen offset:2048\n\t" \
  "v_exp_f32 v183, v183\n\t" \
  "v_exp_f32 v184, v184\n\t" \
  "buffer_load_dwordx4 v[124:127], v224, %[rs], s61 offen offset:3072\n\t" \
  "v_exp_f32 v185, v185\n\t" \
  "v_smfmac_f32_32x32x32_bf16 v[144:159], v[88:91], v[176:183], v222\n\t" \
  "v_exp_f32 v186, v186\n\t" \
  "v_exp_f32 v187, v187\n\t" \
  "v_exp_f32 v188, v188\n\t" \
  "v_exp_f32 v189, v189\n\t" \
  "v_exp_f32 v190, v190\n\t" \
  "v_exp_f32 v191, v191\n\t" \
  "s_mov_b32 m0, s59\n\t" \
  "s_cmp_eq_u32 s72, 7\n\t" \
  "v_smfmac_f32_32x32x32_bf16 v[144:159], v[92:95], v[184:191], v222\n\t" \
  "s_nop 11\n\t" \
  "ds_write_b128 v228, v[144:147] offset:18432\n\t" \
  "ds_write_b128 v228, v[148:151] offset:19456\n\t" \
  "ds_write_addtid_b32 v152 offset:20480\n\t" \
  "s_mov_b64 exec, 1\n\t" \
  "ds_sub_u32 v230, v252 offset:28\n\t" \
  "s_mov_b64 exec, -1\n\t" \
  "s_cbranch_scc1 LRED%=\n\t" \
  "LRB7%=:\n\t" \
  "s_lshl_b32 s68, %[w], 3\n\t" \
  "s_and_b32 s69, s68, 8\n\t" \
  "s_lshl_b32 s68, s68, 6\n\t" \
  "s_and_b32 s68, s68, 1024\n\t" \
  "s_add_u32 s68, s68, s69\n\t" \
  "s_add_u32 s69, %[lds], 18432\n\t" \
  "s_add_u32 s68, s68, s69\n\t" \
  "v_add_u32 v160, s68, v224\n\t" \
  "v_lshrrev_b32 v161, 2, v224\n\t" \
  "v_and_b32 v161, 0x7c, v161\n\t" \
  "v_add_u32 v161, s69, v161\n\t" \
  "s_lshl_b32 s68, %[w], 2\n\t" \
  "s_add_u32 s68, s68, %[lds]\n\t" \
  "s_add_u32 s68, s68, 158720\n\t" \
  "v_add_u32 v162, s68, v224\n\t" \
  "s_cmp_lt_u32 %[w], 4\n\t" \
  "s_waitcnt vmcnt(0) lgkmcnt(0)\n\t" \
  "s_barrier\n\t" \
  "s_cbranch_scc0 LT1%=\n\t" \
  "ds_read_b64 v[32:33], v160 offset:0\n\t" \
  "ds_read_b64 v[34:35], v160 offset:2304\n\t" \
  "ds_read_b64 v[36:37], v160 offset:4608\n\t" \
  "ds_read_b64 v[38:39], v160 offset:6912\n\t" \
  "ds_read_b64 v[40:41], v160 offset:9216\n\t" \
  "ds_read_b64 v[42:43], v160 offset:11520\n\t" \
  "ds_read_b64 v[44:45], v160 offset:13824\n\t" \
  "ds_read_b64 v[46:47], v160 offset:16128\n\t" \
  "ds_read2st64_b32 v[48:49], v161 offset0:8 offset1:17\n\t" \
  "ds_read2st64_b32 v[50:51], v161 offset0:26 offset1:35\n\t" \
  "ds_read2st64_b32 v[52:53], v161 offset0:44 offset1:53\n\t" \
  "ds_read2st64_b32 v[54:55], v161 offset0:62 offset1:71\n\t" \
  "ds_read_b128 v[212:215], v227 offset:6144\n\t" \
  "s_waitcnt lgkmcnt(1)\n\t" \
  "v_pk_add_f32 v[48:49], v[48:49], v[50:51]\n\t" \
  "v_pk_add_f32 v[52:53], v[52:53], v[54:55]\n\t" \
  "v_pk_add_f32 v[32:33], v[32:33], v[34:35]\n\t" \
  "v_pk_add_f32 v[48:49], v[48:49], v[52:53]\n\t" \
  "v_pk_add_f32 v[36:37], v[36:37], v[38:39]\n\t" \
  "v_add_f32 v48, v48, v49\n\t" \
  "v_pk_add_f32 v[40:41], v[40:41], v[42:43]\n\t" \
  "v_rcp_f32 v48, v48\n\t" \
  "v_pk_add_f32 v[44:45], v[44:45], v[46:47]\n\t" \
  "v_pk_add_f32 v[32:33], v[32:33], v[36:37]\n\t" \
  "v_pk_add_f32 v[40:41], v[40:41], v[44:45]\n\t" \
  "v_pk_add_f32 v[32:33], v[32:33], v[40:41]\n\t" \
  "v_pk_mul_f32 v[32:33], v[32:33], v[48:49] op_sel_hi:[1,0]\n\t" \
  "v_cvt_pk_bf16_f32 v32, v32, v33\n\t" \
  "ds_write_b32 v162, v32\n\t" \
  "s_waitcnt lgkmcnt(0)\n\t" \
  "s_barrier\n\t" \
  "ds_read_b128 v[208:211], v227 offset:7168\n\t" \
  "v_smfmac_f32_16x16x64_bf16 v[192:195], v[212:215], v[96:103], v229\n\t" \
  "v_smfmac_f32_16x16x64_bf16 v[196:199], v[212:215], v[104:111], v229\n\t" \
  "v_smfmac_f32_16x16x64_bf16 v[200:203], v[212:215], v[112:119], v229\n\t" \
  "v_smfmac_f32_16x16x64_bf16 v[204:207], v[212:215], v[120:127], v229\n\t" \
  "s_branch LT2%=\n\t" \
  "LT1%=:\n\t" \
  "ds_read_b128 v[212:215], v227 offset:6144\n\t" \
  "s_waitcnt lgkmcnt(0)\n\t" \
  "v_smfmac_f32_16x16x64_bf16 v[192:195], v[212:215], v[96:103], v229\n\t" \
  "v_smfmac_f32_16x16x64_bf16 v[196:199], v[212:215], v[104:111], v229\n\t" \
  "v_smfmac_f32_16x16x64_bf16 v[200:203], v[212:215], v[112:119], v229\n\t" \
  "v_smfmac_f32_16x16x64_bf16 v[204:207], v[212:215], v[120:127], v229\n\t" \
  "s_barrier\n\t" \
  "ds_read_b128 v[208:211], v227 offset:7168\n\t" \
  "LT2%=:\n\t" \
  "s_waitcnt lgkmcnt(0)\n\t" \
  "v_smfmac_f32_16x16x64_bf16 v[192:195], v[208:211], v[0:7], v229\n\t" \
  "v_smfmac_f32_16x16x64_bf16 v[196:199], v[208:211], v[8:15], v229\n\t" \
  "v_smfmac_f32_16x16x64_bf16 v[200:203], v[208:211], v[16:23], v229\n\t" \
  "v_smfmac_f32_16x16x64_bf16 v[204:207], v[208:211], v[24:31], v229\n\t" \
  "s_nop 11\n\t" \
  "s_branch LEND%=\n\t" \
  "LRED%=:\n\t" \
  "s_waitcnt lgkmcnt(0)\n\t" \
  "v_mov_b32 v158, s74\n\t" \
  "v_add_u32 v156, s73, v224\n\t" \
  "v_lshrrev_b32 v157, 2, v224\n\t" \
  "v_and_b32 v157, 0x7c, v157\n\t" \
  "v_add_u32 v157, s73, v157\n\t" \
  "s_mov_b32 s69, 0x200000\n\t" \
  "LRW%=:\n\t" \
  "ds_read_b32 v223, v158\n\t" \
  "s_waitcnt lgkmcnt(0)\n\t" \
  "v_readfirstlane_b32 s68, v223\n\t" \
  "s_cmp_ge_u32 s68, 8\n\t" \
  "s_cbranch_scc1 LRG%=\n\t" \
  "s_sleep 1\n\t" \
  "s_sub_u32 s69, s69, 1\n\t" \
  "s_cbranch_scc0 LRW%=\n\t" \
  "LRG%=:\n\t" \
  "ds_read_b128 v[144:147], v156 offset:0\n\t" \
  "ds_read_b128 v[148:151], v156 offset:1024\n\t" \
  "ds_read_b32 v152, v157 offset:2048\n\t" \
  "ds_read_b128 v[176:179], v156 offset:2304\n\t" \
  "ds_read_b128 v[180:183], v156 offset:3328\n\t" \
  "ds_read_b32 v153, v157 offset:4352\n\t" \
  "ds_read_b128 v[184:187], v156 offset:4608\n\t" \
  "ds_read_b128 v[188:191], v156 offset:5632\n\t" \
  "ds_read_b32 v154, v157 offset:6656\n\t" \
  "ds_read_b128 v[212:215], v156 offset:6912\n\t" \
  "ds_read_b128 v[216:219], v156 offset:7936\n\t" \
  "ds_read_b32 v155, v157 offset:8960\n\t" \
  "s_waitcnt lgkmcnt(6)\n\t" \
  "v_pk_add_f32 v[144:145], v[144:145], v[176:177]\n\t" \
  "v_pk_add_f32 v[146:147], v[146:147], v[178:179]\n\t" \
  "v_pk_add_f32 v[148:149], v[148:149], v[180:181]\n\t" \
  "v_pk_add_f32 v[150:151], v[150:151], v[182:183]\n\t" \
  "v_add_f32 v152, v152, v153\n\t" \
  "ds_read_b128 v[176:179], v156 offset:9216\n\t" \
  "ds_read_b128 v[180:183], v156 offset:10240\n\t" \
  "ds_read_b32 v153, v157 offset:11264\n\t" \
  "s_waitcnt lgkmcnt(6)\n\t" \
  "v_pk_add_f32 v[144:145], v[144:145], v[184:185]\n\t" \
  "v_pk_add_f32 v[146:147], v[146:147], v[186:187]\n\t" \
  "v_pk_add_f32 v[148:149], v[148:149], v[188:189]\n\t" \
  "v_pk_add_f32 v[150:151], v[150:151], v[190:191]\n\t" \
  "v_add_f32 v152, v152, v154\n\t" \
  "ds_read_b128 v[184:187], v156 offset:11520\n\t" \
  "ds_read_b128 v[188:191], v156 offset:12544\n\t" \
  "ds_read_b32 v154, v157 offset:13568\n\t" \
  "s_waitcnt lgkmcnt(6)\n\t" \
  "v_pk_add_f32 v[144:145], v[144:145], v[212:213]\n\t" \
  "v_pk_add_f32 v[146:147], v[146:147], v[214:215]\n\t" \
  "v_pk_add_f32 v[148:149], v[148:149], v[216:217]\n\t" \
  "v_pk_add_f32 v[150:151], v[150:151], v[218:219]\n\t" \
  "v_add_f32 v152, v152, v155\n\t" \
  "ds_read_b128 v[212:215], v156 offset:13824\n\t" \
  "ds_read_b128 v[216:219], v156 offset:14848\n\t" \
  "ds_read_b32 v155, v157 offset:15872\n\t" \
  "s_waitcnt lgkmcnt(6)\n\t" \
  "v_pk_add_f32 v[144:145], v[144:145], v[176:177]\n\t" \
  "v_pk_add_f32 v[146:147], v[146:147], v[178:179]\n\t" \
  "v_pk_add_f32 v[148:149], v[148:149], v[180:181]\n\t" \
  "v_pk_add_f32 v[150:151], v[150:151], v[182:183]\n\t" \
  "v_add_f32 v152, v152, v153\n\t" \
  "ds_read_b128 v[176:179], v156 offset:16128\n\t" \
  "ds_read_b128 v[180:183], v156 offset:17152\n\t" \
  "ds_read_b32 v153, v157 offset:18176\n\t" \
  "s_waitcnt lgkmcnt(6)\n\t" \
  "v_pk_add_f32 v[144:145], v[144:145], v[184:185]\n\t" \
  "v_pk_add_f32 v[146:147], v[146:147], v[186:187]\n\t" \
  "v_pk_add_f32 v[148:149], v[148:149], v[188:189]\n\t" \
  "v_pk_add_f32 v[150:151], v[150:151], v[190:191]\n\t" \
  "v_add_f32 v152, v152, v154\n\t" \
  "s_waitcnt lgkmcnt(3)\n\t" \
  "v_pk_add_f32 v[144:145], v[144:145], v[212:213]\n\t" \
  "v_pk_add_f32 v[146:147], v[146:147], v[214:215]\n\t" \
  "v_pk_add_f32 v[148:149], v[148:149], v[216:217]\n\t" \
  "v_pk_add_f32 v[150:151], v[150:151], v[218:219]\n\t" \
  "v_add_f32 v152, v152, v155\n\t" \
  "s_waitcnt lgkmcnt(0)\n\t" \
  "v_pk_add_f32 v[144:145], v[144:145], v[176:177]\n\t" \
  "v_pk_add_f32 v[146:147], v[146:147], v[178:179]\n\t" \
  "v_pk_add_f32 v[148:149], v[148:149], v[180:181]\n\t" \
  "v_pk_add_f32 v[150:151], v[150:151], v[182:183]\n\t" \
  "v_add_f32 v152, v152, v153\n\t" \
  "v_rcp_f32 v152, v152\n\t" \
  "v_add_u32 v156, s75, v224\n\t" \
  "v_mov_b32 v223, 1\n\t" \
  "v_pk_mul_f32 v[144:145], v[144:145], v[152:153] op_sel_hi:[1,0]\n\t" \
  "v_pk_mul_f32 v[146:147], v[146:147], v[152:153] op_sel_hi:[1,0]\n\t" \
  "v_pk_mul_f32 v[148:149], v[148:149], v[152:153] op_sel_hi:[1,0]\n\t" \
  "v_pk_mul_f32 v[150:151], v[150:151], v[152:153] op_sel_hi:[1,0]\n\t" \
  "v_cvt_pk_bf16_f32 v144, v144, v145\n\t" \
  "v_cvt_pk_bf16_f32 v145, v146, v147\n\t" \
  "v_cvt_pk_bf16_f32 v146, v148, v149\n\t" \
  "v_cvt_pk_bf16_f32 v147, v150, v151\n\t" \
  "ds_write_b128 v156, v[144:147]\n\t" \
  "ds_write_b32 v158, v223 offset:32\n\t" \
  "s_waitcnt lgkmcnt(0)\n\t" \
  "s_cmp_eq_u32 s72, 1\n\t" \
  "s_cbranch_scc1 LRB1%=\n\t" \
  "s_cmp_eq_u32 s72, 2\n\t" \
  "s_cbranch_scc1 LRB2%=\n\t" \
  "s_cmp_eq_u32 s72, 3\n\t" \
  "s_cbranch_scc1 LRB3%=\n\t" \
  "s_cmp_eq_u32 s72, 4\n\t" \
  "s_cbranch_scc1 LRB4%=\n\t" \
  "s_cmp_eq_u32 s72, 5\n\t" \
  "s_cbranch_scc1 LRB5%=\n\t" \
  "s_cmp_eq_u32 s72, 6\n\t" \
  "s_cbranch_scc1 LRB6%=\n\t" \
  "s_branch LRB7%=\n\t" \
  "LSPIN2%=:\n\t" \
  "s_mov_b32 s69, 0x200000\n\t" \
  "LSW2%=:\n\t" \
  "s_sleep 1\n\t" \
  "ds_read_b32 v223, v230 offset:32\n\t" \
  "s_waitcnt lgkmcnt(0)\n\t" \
  "v_readfirstlane_b32 s68, v223\n\t" \
  "s_cmp_lg_u32 s68, 0\n\t" \
  "s_cbranch_scc1 LBACK2%=\n\t" \
  "s_sub_u32 s69, s69, 1\n\t" \
  "s_cbranch_scc0 LSW2%=\n\t" \
  "s_branch LBACK2%=\n\t" \
  "LSPIN3%=:\n\t" \
  "s_mov_b32 s69, 0x200000\n\t" \
  "LSW3%=:\n\t" \
  "s_sleep 1\n\t" \
  "ds_read_b32 v223, v230 offset:36\n\t" \
  "s_waitcnt lgkmcnt(0)\n\t" \
  "v_readfirstlane_b32 s68, v223\n\t" \
  "s_cmp_lg_u32 s68, 0\n\t" \
  "s_cbranch_scc1 LBACK3%=\n\t" \
  "s_sub_u32 s69, s69, 1\n\t" \
  "s_cbranch_scc0 LSW3%=\n\t" \
  "s_branch LBACK3%=\n\t" \
  "LSPIN4%=:\n\t" \
  "s_mov_b32 s69, 0x200000\n\t" \
  "LSW4%=:\n\t" \
  "s_sleep 1\n\t" \
  "ds_read_b32 v223, v230 offset:40\n\t" \
  "s_waitcnt lgkmcnt(0)\n\t" \
  "v_readfirstlane_b32 s68, v223\n\t" \
  "s_cmp_lg_u32 s68, 0\n\t" \
  "s_cbranch_scc1 LBACK4%=\n\t" \
  "s_sub_u32 s69, s69, 1\n\t" \
  "s_cbranch_scc0 LSW4%=\n\t" \
  "s_branch LBACK4%=\n\t" \
  "LSPIN5%=:\n\t" \
  "s_mov_b32 s69, 0x200000\n\t" \
  "LSW5%=:\n\t" \
  "s_sleep 1\n\t" \
  "ds_read_b32 v223, v230 offset:44\n\t" \
  "s_waitcnt lgkmcnt(0)\n\t" \
  "v_readfirstlane_b32 s68, v223\n\t" \
  "s_cmp_lg_u32 s68, 0\n\t" \
  "s_cbranch_scc1 LBACK5%=\n\t" \
  "s_sub_u32 s69, s69, 1\n\t" \
  "s_cbranch_scc0 LSW5%=\n\t" \
  "s_branch LBACK5%=\n\t" \
  "LSPIN6%=:\n\t" \
  "s_mov_b32 s69, 0x200000\n\t" \
  "LSW6%=:\n\t" \
  "s_sleep 1\n\t" \
  "ds_read_b32 v223, v230 offset:48\n\t" \
  "s_waitcnt lgkmcnt(0)\n\t" \
  "v_readfirstlane_b32 s68, v223\n\t" \
  "s_cmp_lg_u32 s68, 0\n\t" \
  "s_cbranch_scc1 LBACK6%=\n\t" \
  "s_sub_u32 s69, s69, 1\n\t" \
  "s_cbranch_scc0 LSW6%=\n\t" \
  "s_branch LBACK6%=\n\t" \
  "LSPIN7%=:\n\t" \
  "s_mov_b32 s69, 0x200000\n\t" \
  "LSW7%=:\n\t" \
  "s_sleep 1\n\t" \
  "ds_read_b32 v223, v230 offset:52\n\t" \
  "s_waitcnt lgkmcnt(0)\n\t" \
  "v_readfirstlane_b32 s68, v223\n\t" \
  "s_cmp_lg_u32 s68, 0\n\t" \
  "s_cbranch_scc1 LBACK7%=\n\t" \
  "s_sub_u32 s69, s69, 1\n\t" \
  "s_cbranch_scc0 LSW7%=\n\t" \
  "s_branch LBACK7%=\n\t" \
  "LBAD%=:\n\t" \
  "s_nop 15\n\t" \
  "s_setprio 0\n\t" \
  "LEND%=:\n\t" \
  "s_waitcnt vmcnt(0) lgkmcnt(0)\n\t" \
  "s_mov_b32 m0, s70\n\t"

#define ATTN_CLOBBERS "v4", "v5", "v6", "v7", "v8", "v9", "v10", "v11", "v12", "v13", "v14", "v15", "v16", "v17", "v18", "v19", "v20", "v21", "v22", "v23", "v24", "v25", "v26", "v27", "v28", "v29", "v30", "v31", "v36", "v37", "v38", "v39", "v40", "v41", "v42", "v43", "v44", "v45", "v46", "v47", "v48", "v49", "v50", "v51", "v52", "v53", "v54", "v55", "v56", "v57", "v58", "v59", "v60", "v61", "v62", "v63", "v64", "v65", "v66", "v67", "v68", "v69", "v70", "v71", "v72", "v73", "v74", "v75", "v76", "v77", "v78", "v79", "v80", "v81", "v82", "v83", "v84", "v85", "v86", "v87", "v88", "v89", "v90", "v91", "v92", "v93", "v94", "v95", "v96", "v97", "v98", "v99", "v100", "v101", "v102", "v103", "v104", "v105", "v106", "v107", "v108", "v109", "v110", "v111", "v112", "v113", "v114", "v115", "v116", "v117", "v118", "v119", "v120", "v121", "v122", "v123", "v124", "v125", "v126", "v127", "v128", "v129", "v130", "v131", "v132", "v133", "v134", "v135", "v136", "v137", "v138", "v139", "v140", "v141", "v142", "v143", "v148", "v149", "v150", "v151", "v152", "v153", "v154", "v155", "v156", "v157", "v158", "v159", "v160", "v161", "v162", "v163", "v164", "v165", "v166", "v167", "v168", "v169", "v170", "v171", "v172", "v173", "v174", "v175", "v180", "v181", "v182", "v183", "v184", "v185", "v186", "v187", "v188", "v189", "v190", "v191", "v212", "v213", "v214", "v215", "v216", "v217", "v218", "v219", "v220", "v221", "v222", "v223", "v224", "v225", "v226", "v227", "v228", "v229", "v230", "v231", "v252", "v253", "v254", "v255", "s59", "s60", "s61", "s62", "s63", "s64", "s65", "s66", "s67", "s68", "s69", "s70", "s71", "s72", "s73", "s74", "s75", "s76", "s77", "s78", "s79", "vcc", "scc", "memory"
static_assert(PART == 2304 && NPF == 14 && B_WP == 36864 && B_OF == 151552 && B_FL == 160256 && WS_WO == 524288 && L_K == 2097152 && L_V == 2162688 && L_KN == 2228224 && B_ST == 159744 && L_QF == 0 && FL_G == 131072, "constants of the attention loop");

DEV void attn_publish(char* sm, int t, int slot, int w, int l, const f32x16& o, float last) {
  char* pp = sm + slot * PSLOT + w * PART;
  *(LAS f32x4*)(pp + l * 16) = f32x4{o[0], o[1], o[2], o[3]};
  *(LAS f32x4*)(pp + 1024 + l * 16) = f32x4{o[4], o[5], o[6], o[7]};
  *(LAS float*)(pp + 2048 + l * 4) = last;
  asm volatile("s_waitcnt lgkmcnt(0)" ::: "memory");
  if (l == 0) { unsigned a = (unsigned)(uintptr_t)(LAS char*)(sm + B_FL + t * 4); asm volatile("ds_add_u32 %0, %1" :: "v"(a), "v"(1u) : "memory"); }
}

#define WLOAD(e) bld(rw, l * 16 + (((e) >> 1) & 3) * 1024, (((e) & 1) ? wgb : wga) + ((e) >> 3) * 4096)
DEV void attn_safe(char* sm, rsrc_t rw, rsrc_t rx, unsigned lay, unsigned wga, unsigned wgb, int lin, int w, f32x4 (&acc)[4], u32x16& wr16, u32x4& wr4) {
  const int l = lane_id();
  u32x4 kf[8], vf[16];
#pragma unroll
  for (int T = 0; T < 8; T++) {
    kf[T] = bld1(rw, l * 16 + (T & 3) * 1024, lay + L_K + w * 8192 + (T >> 2) * 4096);
    vf[2 * T] = bld1(rw, l * 16 + (T & 3) * 1024, lay + L_V + w * 8192 + (T >> 2) * 4096);
  }
  const unsigned onev = (l & 15) == 0 ? 0x3F803F80u : 0u;
#pragma unroll
  for (int T = 0; T < 8; T++)
#pragma unroll
    for (int e = 0; e < 4; e++) { unsigned a = vf[2 * T][e], y = onev; sw16(a, y); vf[2 * T][e] = a; vf[2 * T + 1][e] = y; }
  const unsigned wo0 = WS_WO + w * 8192;
#pragma unroll 1
  for (int t = 0; t < 8; t++) {
    u32x4 q = lds128(sm + B_OF + t * 1024 + l * 16);
    const f32x16 z = {};
    float m = -3.0e38f;
#pragma unroll
    for (int T = 0; T < 8; T++) {
      f32x16 s = mfma32(kf[T], q, z);
#pragma unroll
      for (int i = 0; i < 16; i++) m = fmaxf(m, s[i]);
      SBAR();
    }
    m = max32(m);
    asm volatile("" : "+v"(q));
    f32x16 o = {};
#pragma unroll
    for (int T = 0; T < 8; T++) {
      f32x16 s = mfma32(kf[T], q, z);
      f32x8 p0, p1;
#pragma unroll
      for (int i = 0; i < 8; i++) { p0[i] = __builtin_amdgcn_exp2f(s[i] - m); p1[i] = __builtin_amdgcn_exp2f(s[8 + i] - m); }
      o = __builtin_amdgcn_smfmac_f32_32x32x32_bf16(__builtin_bit_cast(bf16x8, vf[2 * T]), __builtin_bit_cast(bf16x16, p0), o, 0xDDDDDDDD, 0, 0);
      o = __builtin_amdgcn_smfmac_f32_32x32x32_bf16(__builtin_bit_cast(bf16x8, vf[2 * T + 1]), __builtin_bit_cast(bf16x16, p1), o, 0xDDDDDDDD, 0, 0);
      SBAR();
    }
    attn_publish(sm, t, t, w, l, o, l >= 32 ? m : o[8]);
  }
  attn_reduce(sm, w, w, l);
#pragma unroll
  for (int r = 0; r < 4; r++) {
    const u32x2 xr = __builtin_amdgcn_raw_buffer_load_b64(rx, l < 32 ? (4 * (l >> 4) + r) * 1024 + (l & 7) * 16 + (l & 8) : 0x80000000u, lin * 8 * D * 2 + w * 128, 16);
    acc[0][r] = bflo(xr[0]); acc[1][r] = bfhi(xr[0]); acc[2][r] = bflo(xr[1]); acc[3][r] = bfhi(xr[1]);
  }
#pragma unroll 1
  for (int t = 0; t < 8; t++) {
    u32x4 wf[8];
#pragma unroll
    for (int i = 0; i < 8; i++) wf[i] = bld(rw, l * 16 + (i & 3) * 1024, wo0 + t * 65536 + (i >> 2) * 4096);
    attn_oproj(sm, t, l, wf, acc);
  }
  {
    const unsigned lds0 = (unsigned)(uintptr_t)(LAS char*)sm;
#pragma unroll 1
    for (int e = 0; e < NPF; e++) bdma16(rw, l * 16, ((e & 1) ? wgb : wga) + (e >> 1) * 1024, lds0 + B_WP + (w * NPF + e) * 1024);
  }
#pragma unroll
  for (int i = 0; i < 4; i++) { const u32x4 t = WLOAD(NPF + i); wr16[4 * i] = t[0]; wr16[4 * i + 1] = t[1]; wr16[4 * i + 2] = t[2]; wr16[4 * i + 3] = t[3]; }
  wr4 = WLOAD(NPF + 4);
  asm volatile("s_waitcnt vmcnt(0)" ::: "memory");
}

__global__ void __launch_bounds__(512, 2) k_main(const u16* x, char* ws, u16* out) {
  __shared__ __attribute__((aligned(16))) char sm[B_LDS];
  const int w0 = __builtin_amdgcn_readfirstlane(threadIdx.x >> 6);
  const rsrc_t rw = mkrs(ws, WS_END);
#pragma unroll 1
  for (int L = 0; L < 2; L++) {
    int b = blockIdx.x, w = w0; asm volatile("" : "+s"(b), "+s"(w));
    __builtin_assume(w >= 0 && w < 8 && b >= 0 && b < 256);
    int l, lin = (b & 7) * 32 + (b >> 3);
    const unsigned lay = WS_LAY + L * L_SZ;
    u32x4 f_k, f_v, f_n, f_qs, f_q;
    asm volatile(ATTN_FIRST : "=&{v[0:3]}"(f_k), "=&{v[32:35]}"(f_v), "=&{v[144:147]}"(f_n), "=&{v[176:179]}"(f_qs), "=&{v[208:211]}"(f_q), [lin] "+s"(lin), [w] "+s"(w)
                 : [rs] "s"(rw), [lay] "s"(lay), [gw] "s"(L) : ATTN_FIRST_CLOBBERS);
    __builtin_assume(w >= 0 && w < 8 && lin >= 0 && lin < 256);
    const int cl = lin >> 4;
    const int hba = (lin & 15) * 14 + (w < 4 ? 2 * w : 4 + w), hbb = (lin & 15) * 14 + (w < 4 ? 2 * w + 1 : 10 + (w >> 1));
    const unsigned wga = WS_WGU + hba * 16384, wgb = WS_WGU + hbb * 16384;
    u32x16 wr16; u32x4 wr4;
    {
      const int rg = lin;
      const rsrc_t rx = L == 0 ? mkrs(x, S * D * 2) : mkrs(ws + WS_X2, S * D * 2);
      const unsigned sph = lay + L_H + (cl * 8 + w) * 57344 + (lin & 15) * 3584;
      const unsigned spx = L == 0 ? WS_X2 + ((cl * 128 + (w >> 2) * 64 + ((lin >> 3) & 1) * 32 + (w & 3) * 8) * D + (lin & 7) * 64) * 2 : 0x80000000u;
      const unsigned spn = lay + L_XN1 + ((rg >> 1) * 16 + 2 * w) * 1024 + (rg & 1) * 128;
      int fast; f32x16 a16;
      asm volatile(ATTN_LOOP : "=&{v[192:207]}"(a16), "=&{v[232:247]}"(wr16), "=&{v[248:251]}"(wr4), [fast] "=&s"(fast),
                     "+{v[0:3]}"(f_k), "+{v[32:35]}"(f_v), "+{v[144:147]}"(f_n), "+{v[176:179]}"(f_qs), "+{v[208:211]}"(f_q)
                   : [rs] "s"(rw), [rx] "s"(rx), [w] "s"(w), [lds] "s"((unsigned)(uintptr_t)(LAS char*)sm), [lay] "s"(lay), [nsh] "s"(-SHIFT), [lim2] "s"(FAST_LIMIT * FAST_LIMIT),
                     [sx] "s"(rg * 8 * D * 2 + w * 128), [swa] "s"(wga), [swb] "s"(wgb),
                     [sph] "s"(sph), [spx] "s"(spx), [spn] "s"(spn)
                   : ATTN_CLOBBERS);
      f32x4 acc[4];
#pragma unroll
      for (int i = 0; i < 4; i++) acc[i] = f32x4{a16[4 * i], a16[4 * i + 1], a16[4 * i + 2], a16[4 * i + 3]};
      if (__builtin_expect(!fast, 0)) {
        l = lane_id();
        const u32x4 mk = {~0u, ~0u, ~0u, ~0u};
        bst1(mk, rw, l * 16, sph); bst1(mk, rw, l * 16 + 1024, sph); bst1(mk, rw, l * 16 + 2048, sph); bst1(mk, rw, l * 16 + 2560, sph); bst1(mk, rw, (l >> 3) * 1024 + (l & 7) * 16, spx); bst1(mk, rw, (l >> 3) * 256 + (l & 7) * 16, spn);
        attn_safe(sm, rw, rx, lay, wga, wgb, lin, w, acc, wr16, wr4);
      }
      l = lane_id();
      float y[2][4];
#pragma unroll
      for (int nt = 0; nt < 4; nt++)
#pragma unroll
        for (int r = 0; r < 2; r++) { unsigned a = __float_as_uint(acc[nt][r]), bq = __float_as_uint(acc[nt][r + 2]); sw32(a, bq); y[r][nt] = __uint_as_float(a) + __uint_as_float(bq); }
      const int p00 = pack2(y[0][0], y[0][1]), p01 = pack2(y[0][2], y[0][3]), p10 = pack2(y[1][0], y[1][1]), p11 = pack2(y[1][2], y[1][3]);
      const u32x4 x1 = {(unsigned)__builtin_amdgcn_update_dpp(p00, p10, 0x128, 0xF, 0xC, false), (unsigned)__builtin_amdgcn_update_dpp(p01, p11, 0x128, 0xF, 0xC, false),
                        (unsigned)__builtin_amdgcn_update_dpp(p10, p00, 0x128, 0xF, 0x3, false), (unsigned)__builtin_amdgcn_update_dpp(p11, p01, 0x128, 0xF, 0x3, false)};
      const int ri = ((l >> 4) & 1) * 4 + (l >> 5) * 2 + ((l >> 3) & 1);
      float sum = dot2(x1[0], 0x3F803F80u, dot2(x1[1], 0x3F803F80u, 0.f)) + dot2(x1[2], 0x3F803F80u, dot2(x1[3], 0x3F803F80u, 0.f));
      float sq = dot2(x1[0], x1[0], dot2(x1[1], x1[1], 0.f)) + dot2(x1[2], x1[2], dot2(x1[3], x1[3], 0.f));
      sum += dppf<0xB1>(sum); sq += dppf<0xB1>(sq); sum += dppf<0x4E>(sum); sq += dppf<0x4E>(sq); sum += dppf<0x141>(sum); sq += dppf<0x141>(sq);
      *(LAS f32x2*)(sm + B_ST + (ri * 8 + w) * 8) = f32x2{sum, sq};
      asm volatile("s_waitcnt lgkmcnt(0)\n\ts_barrier" ::: "memory");
      f32x4 t4[4];
#pragma unroll
      for (int i = 0; i < 4; i++) t4[i] = *(const LAS f32x4*)(sm + B_ST + ri * 64 + i * 16);
      SBAR();
      if (w == SW && l < 16) flag_add(rw, FL_B + L * 65536 + (cl * 16 + l) * 256);
      float v[8];
#pragma unroll
      for (int e = 0; e < 4; e++) { v[2 * e] = bflo(x1[e]); v[2 * e + 1] = bfhi(x1[e]); }
      unsigned xno = (l & 7) * 256 + ri * 16 + lay + L_XN1 + ((rg >> 1) * 16 + 2 * w) * 1024 + (rg & 1) * 128;
      asm volatile("" : "+v"(v[0]), "+v"(v[1]), "+v"(v[2]), "+v"(v[3]), "+v"(v[4]), "+v"(v[5]), "+v"(v[6]), "+v"(v[7]), "+v"(xno));
      SBAR();
      const f32x4 ts = (t4[0] + t4[1]) + (t4[2] + t4[3]);
      float mean, rinv; ln_scalars(ts[0] + ts[2], ts[1] + ts[3], mean, rinv);
      const float nb = -mean * rinv;
      u32x4 xn;
#pragma unroll
      for (int e = 0; e < 4; e++) xn[e] = pack2(fmaf(v[2 * e], rinv, nb), fmaf(v[2 * e + 1], rinv, nb));
      bst1(nomark(xn), rw, xno, 0);
      SBAR();
      bst1(x1, rw, xno - (L_XN1 - L_X1), 0);
    }
    const int da = (lin >> 3) & 1, dcb = lin & 7;
    l = lane_id();
    const unsigned xo = lay + L_XN1 + cl * 131072;
    u32x4 WQ[16];
    const unsigned wqo = L == 0 ? WS_WQ + (8 * (dcb & 3) + w) * 16384 : 0x80000000u;
#define QLOAD(e) bld(rw, l * 16 + ((e) & 3) * 1024, wqo + ((e) >> 2) * 4096)
    u32x4 xr;
#define GOFF(l_) (((cl * 128 + (w >> 2) * 64 + da * 32 + (w & 3) * 8 + ((l_) >> 3)) * D + dcb * 64 + ((l_) & 7) * 8) * 2)
    {
      u32x4 W[32];
      asm volatile("s_setprio 3" ::: "memory");
#pragma unroll
      for (int e = 0; e < NPF; e++) W[e] = lds128(sm + B_WP + (w * NPF + e) * 1024 + l * 16);
#pragma unroll
      for (int e = 0; e < 4; e++) W[NPF + e] = u32x4{wr16[4 * e], wr16[4 * e + 1], wr16[4 * e + 2], wr16[4 * e + 3]};
      W[NPF + 4] = wr4;
      SBAR();
      if (w == PW) flag_wait(rw, FL_B + L * 65536 + lin * 256, 16u);
      asm volatile("s_waitcnt lgkmcnt(0)\n\ts_barrier" ::: "memory");
      u32x4 WD[28], hf[28];
      const unsigned wdo = WS_WD + (dcb * 56 + 7 * w) * 4096;
      const unsigned ho = lay + L_H + (cl * 8 + 2 * da) * 57344 + 7 * w * 1024;
#define DLOAD(e) bld(rw, l * 16 + ((e) & 3) * 1024, wdo + ((e) >> 2) * 4096)
#define HLOAD(e) bld1(rw, l * 16 + (((e) % 7) & 3) * 1024, ho + ((((e) / 7) >> 1) * 4 + (((e) / 7) & 1)) * 57344 + (((e) % 7) >> 2) * 4096)
      LAS char* xa = (LAS char*)sm + l * 16; LAS char* xbp = xa + 65536;
      asm volatile("" : "+v"(xbp));
      u32x4 xf[2][4], xs[3][4];
#define XOFF(c) (xo + ((c) >> 1) * 65536 + (((c) & 1) * 8 + w) * 1024)
#define XGET(dst, c) _Pragma("unroll") for (int t = 0; t < 4; t++) dst[t] = bld1(rw, l * 16, XOFF(c) + t * 16384)
#define XW1(src, c, t) *(LAS u32x4*)(((c) < 2 ? xa : xbp) + ((c) & 1) * 32768 + (w * 4 + (t)) * 1024) = src[t]
#define XFIX(c) do { if (__builtin_expect(!fresh(xm), 0)) { _Pragma("unroll 1") for (int tr = 0; tr < RETRY; tr++) { __builtin_amdgcn_s_sleep(XSLEEP); asm volatile("" ::: "memory"); \
          u32x4 xt[4]; XGET(xt, c); _Pragma("unroll") for (int t = 0; t < 4; t++) XW1(xt, c, t); \
          if (fresh(umax4(xt[3], umax4(xt[2], umax4(xt[1], umax4(xt[0], 0u)))))) break; } } } while (0)
      unsigned xm;
      XGET(xf[0], 0);
      SBAR();
#pragma unroll
      for (int t = 0; t < 4; t++) XW1(xf[0], 0, t);
      SBAR();
      XGET(xs[0], 1);
      SBAR();
      xm = umax4(xf[0][3], umax4(xf[0][2], umax4(xf[0][1], umax4(xf[0][0], 0u))));
      XFIX(0);
      asm volatile("s_waitcnt lgkmcnt(0)\n\ts_barrier" ::: "memory");
#define XREAD(g) _Pragma("unroll") for (int t = 0; t < 4; t++) xf[(g) % 2][t] = *(const LAS u32x4*)(((g) < 16 ? xa : xbp) + (((g) & 15) * 4 + t) * 1024)
      XREAD(0);
      SBAR();
      unsigned seen = 0;
#define SLOT(i) do { \
        if ((i) < 2) W[19 + (i)] = WLOAD(19 + (i)); \
        else if ((i) < 6) xs[1][(i) - 2] = bld1(rw, l * 16, XOFF(2) + ((i) - 2) * 16384); \
        else if ((i) < 17) W[15 + (i)] = WLOAD(15 + (i)); \
        else if ((i) < 21) xs[2][(i) - 17] = bld1(rw, l * 16, XOFF(3) + ((i) - 17) * 16384); \
        else if ((i) < 32) { if ((i) >= 32 - WDE) WD[(i) - (32 - WDE)] = DLOAD((i) - (32 - WDE)); } \
        else if ((i) - 32 + WDE < 28) WD[(i) - 32 + WDE] = DLOAD((i) - 32 + WDE); \
        else if ((i) - 60 + WDE < EW) hf[(i) - 60 + WDE] = HLOAD((i) - 60 + WDE); \
        if ((i) >= P1 && (i) < P1 + 4) { XW1(xs[0], 1, (i) - P1); xm = umax4(xs[0][(i) - P1], (i) == P1 ? 0u : xm); } \
        if ((i) == P1 + 3) { XFIX(1); lds_arrive(sm + FW_S); } \
        if ((i) >= P2 && (i) < P2 + 4) { XW1(xs[1], 2, (i) - P2); xm = umax4(xs[1][(i) - P2], (i) == P2 ? 0u : xm); } \
        if ((i) == P2 + 3) { XFIX(2); lds_arrive(sm + FW_S + 4); } \
        if ((i) >= P3 && (i) < P3 + 4) { XW1(xs[2], 3, (i) - P3); xm = umax4(xs[2][(i) - P3], (i) == P3 ? 0u : xm); } \
        if ((i) == P3 + 3) { XFIX(3); lds_arrive(sm + FW_S + 8); } \
        SBAR(); } while (0)
      static_assert(NPF + NRG == 19, "slot table");
#pragma unroll
      for (int p = 0; p < 2; p++) {
        f32x4 acc[2][4] = {};
        const int dual = ((p == 0 ? 0x5F : 0xAF) >> w) & 1;
#pragma unroll
        for (int ks = 0; ks < 16; ks++) {
          const int g = p * 16 + ks;
          if (g + 1 < 32) {
            if (((g + 1) & 7) == 0) { if (__builtin_amdgcn_readfirstlane(seen) < 8u) lds_wait_ge(sm + FW_S + (g >> 3) * 4, 8u); asm volatile("" ::: "memory"); }
            if ((g & 7) == 4 && g < 24) seen = lds_peek(sm + FW_S + (g >> 3) * 4);
          }
          SBAR();
#pragma unroll
          for (int t = 0; t < 4; t++) {
            acc[0][t] = mfma16(W[2 * ks], xf[g % 2][t], acc[0][t]);
            SBAR();
            if (g + 1 < 32) xf[(g + 1) % 2][t] = *(const LAS u32x4*)(((g + 1) < 16 ? xa : xbp) + (((g + 1) & 15) * 4 + t) * 1024);
            SBAR();
          }
          SLOT(2 * g);
          asm volatile("s_cmp_eq_u32 %9, 0\n\ts_cbranch_scc1 3%=f\n\t"
                       "v_mfma_f32_16x16x32_bf16 %0, %4, %5, %0\n\tv_mfma_f32_16x16x32_bf16 %1, %4, %6, %1\n\t"
                       "v_mfma_f32_16x16x32_bf16 %2, %4, %7, %2\n\tv_mfma_f32_16x16x32_bf16 %3, %4, %8, %3\n3%=:"
                       : "+v"(acc[1][0]), "+v"(acc[1][1]), "+v"(acc[1][2]), "+v"(acc[1][3])
                       : "v"(W[2 * ks + 1]), "v"(xf[g % 2][0]), "v"(xf[g % 2][1]), "v"(xf[g % 2][2]), "v"(xf[g % 2][3]), "s"(dual) : "scc");
          SBAR();
          SLOT(2 * g + 1);
          if (p == 1 && ks == PS1) __builtin_amdgcn_s_setprio(1);
          if (p == 1 && ks == PS2) __builtin_amdgcn_s_setprio(0);
        }
        asm volatile("s_nop 7" : "+v"(acc[1][0]), "+v"(acc[1][1]), "+v"(acc[1][2]), "+v"(acc[1][3]));
        const int l2 = lane_id();
        const unsigned hst = (l2 >> 4) * 57344 + (l2 & 15) * 16;
#pragma unroll
        for (int q = 0; q < 2; q++) {
          unsigned d0 = pack2(acc[q][0][0] * acc[q][0][2], acc[q][0][1] * acc[q][0][3]), d1 = pack2(acc[q][1][0] * acc[q][1][2], acc[q][1][1] * acc[q][1][3]);
          unsigned d2 = pack2(acc[q][2][0] * acc[q][2][2], acc[q][2][1] * acc[q][2][3]), d3 = pack2(acc[q][3][0] * acc[q][3][2], acc[q][3][1] * acc[q][3][3]);
          sw16(d0, d1); sw16(d2, d3); sw32(d0, d2); sw32(d1, d3);
          bst1(nomark(u32x4{d0, d1, d2, d3}), rw, hst, (q == 0 || dual) ? lay + L_H + (cl * 8 + 4 * p) * 57344 + (q ? hbb : hba) * 256 : 0x80000000u);
        }
        asm volatile("" ::: "memory");
        if (p == 0) __builtin_amdgcn_s_setprio(2);
      }
      f32x4 acc[2][4];
      LAS char* d0p; LAS char* d1p;
      { const int l2 = lane_id(); d0p = (LAS char*)sm + w * 8192 + (l2 >> 4) * 256 + (((l2 & 15) ^ (4 * (l2 >> 4))) * 16); d1p = (LAS char*)sm + w * 8192 + (l2 >> 4) * 256 + (((l2 & 15) ^ (4 * (l2 >> 4) + 2)) * 16); }
#define DST1(i, ct) *(LAS f32x4*)(((ct) < 2 ? d0p : d1p) + ((i) >> 1) * 65536 + (((i) & 1) * 4 + (ct)) * 1024) = acc[(i) & 1][ct]
#define DSTEP(H, i, s) do { _Pragma("unroll") for (int ct = 0; ct < 4; ct++) acc[(i) & 1][ct] = mfma16(WD[(s) * 4 + ct], H, (s) == 0 ? f32x4{} : acc[(i) & 1][ct]); SBAR(); } while (0)
      static_assert(14 - EW <= 8, "the gate precedes the first write of partial sums");
#pragma unroll
      for (int i = 0; i < 4; i++) {
#pragma unroll
        for (int s = 0; s < 7; s++) {
          const int e = i * 7 + s;
          DSTEP(hf[e], i, s);
          if (e + EW == 14) {
            lds_arrive(sm + FW_G);
            lds_wait_ge(sm + FW_G, 8u);
          }
          if (e + EW < 28) hf[e + EW] = HLOAD(e + EW);
          if (e >= 28 - QD) WQ[e - (28 - QD)] = QLOAD(e - (28 - QD));
          if (i > 0 && s >= 1 && s < 5) DST1(i - 1, s - 1);
          if (e == XRE) {
            lds_wait_ge(sm + FW_H, 8u);
            const int l3 = lane_id();
            xr = bld1(rw, (l3 & 7) * 256 + (l3 >> 3) * 16, lay + L_X1 + ((cl * 8 + (w >> 2) * 4 + da * 2 + ((w >> 1) & 1)) * 16 + 2 * dcb) * 1024 + (w & 1) * 128);
          }
          SBAR();
        }
        if (__builtin_expect(__any(acc[i & 1][0][0] != acc[i & 1][0][0]), 0)) {
#pragma unroll 1
          for (int tr = 0; tr < RETRY; tr++) {
            asm volatile("" ::: "memory");
            unsigned mk = 0u; u32x4 ht[7];
#pragma unroll
            for (int s = 0; s < 7; s++) ht[s] = HLOAD(i * 7 + s);
            SBAR();
#pragma unroll
            for (int s = 0; s < 7; s++) { DSTEP(ht[s], i, s); mk = umax4(ht[s], mk); }
            if (fresh(mk)) break;
            __builtin_amdgcn_s_sleep(XSLEEP);
          }
        }
        if (i == 0) lds_arrive(sm + FW_H);
      }
#pragma unroll
      for (int ct = 0; ct < 4; ct++) DST1(3, ct);
      SBAR();
    }
    {
      l = lane_id();
      const int p = w >> 2, u = (w >> 1) & 1, tk = (w & 1) * 8 + (l >> 3), ch = l & 7;
      const unsigned go = GOFF(l);
      const LAS char* pp = (const LAS char*)sm + p * 65536 + (u * 4 + 2 * (ch & 1)) * 1024 + (ch >> 1) * 256 + ((tk ^ (2 * ch)) * 16);
      asm volatile("s_waitcnt lgkmcnt(0)\n\ts_barrier" : "+v"(pp) :: "memory");
      f32x4 pa[8], pb[8];
#pragma unroll
      for (int ww = 0; ww < 8; ww++) { pa[ww] = *(const LAS f32x4*)(pp + ww * 8192); pb[ww] = *(const LAS f32x4*)(pp + ww * 8192 + 1024); }
      SBAR();
      const f32x4 r0 = ((pa[0] + pa[1]) + (pa[2] + pa[3])) + ((pa[4] + pa[5]) + (pa[6] + pa[7])), r1 = ((pb[0] + pb[1]) + (pb[2] + pb[3])) + ((pb[4] + pb[5]) + (pb[6] + pb[7]));
      const u32x4 o = {pack2(r0[0] + bflo(xr[0]), r0[1] + bfhi(xr[0])), pack2(r0[2] + bflo(xr[1]), r0[3] + bfhi(xr[1])),
                       pack2(r1[0] + bflo(xr[2]), r1[1] + bfhi(xr[2])), pack2(r1[2] + bflo(xr[3]), r1[3] + bfhi(xr[3]))};
      if (L == 1) { __builtin_amdgcn_raw_buffer_store_b128(o, mkrs(out, S * D * 2), go, 0, 16);
        break; }
      bst1(nomark(o), rw, go, WS_X2);
    }
    {
      const unsigned nl = WS_LAY + L_SZ;
      const int T = cl * 4 + 2 * (dcb >> 2) + da, j = dcb & 3, head = 8 * j + w;
      u32x4 WJ[16];
      asm volatile("s_barrier" ::: "memory");
      const bool isV = (j & 2) != 0;
      const f32x4 cs = __builtin_bit_cast(f32x4, bld(rw, (l >> 4) * 16, WS_CS + head * 64));
      const f32x4 cj = __builtin_bit_cast(f32x4, bld(rw, isV ? 64 + (l & 15) * 4 : (l >> 4) * 16, WS_CS + 2048));
      if (w == JW) {
#pragma unroll
        for (int e = 0; e < 16; e++) WJ[e] = bld(rw, l * 16 + (e & 3) * 1024, WS_WKV + (j >> 1) * 16384 + (e >> 2) * 4096);
      } else {
#pragma unroll
        for (int e = 0; e < 16; e++) asm volatile("" : "=v"(WJ[e]));
      }
#pragma unroll
      for (int e = QD; e < QPRE; e++) WQ[e] = QLOAD(e);
      asm volatile("s_barrier" ::: "memory");
      const int row = 4 * w + (l >> 4), c16 = l & 15;
      u32x4 xv[4];
#pragma unroll
      for (int i = 0; i < 4; i++) xv[i] = bld1(rw, row * 1024 + c16 * 16 + i * 256, WS_X2 + T * 32768);
      asm volatile("s_barrier" ::: "memory");
#pragma unroll
      for (int e = QPRE; e < 16; e++) WQ[e] = QLOAD(e);
      SBAR();
      float sum, sq;
#define AROWS() do { _Pragma("unroll") for (int i = 0; i < 4; i++) *(LAS u32x4*)(sm + (row >> 4) * 16384 + (c16 + 16 * i) * 256 + (((row & 15) ^ (c16 & 7)) * 16)) = xv[i]; \
        SBAR(); float sum2 = 0.f, sq2 = 0.f; sum = 0.f; sq = 0.f; \
        _Pragma("unroll") for (int i = 0; i < 4; i++) _Pragma("unroll") for (int e = 0; e < 4; e += 2) { sum = dot2(xv[i][e], 0x3F803F80u, sum); sq = dot2(xv[i][e], xv[i][e], sq); \
          sum2 = dot2(xv[i][e + 1], 0x3F803F80u, sum2); sq2 = dot2(xv[i][e + 1], xv[i][e + 1], sq2); } \
        sum += sum2; sq += sq2; } while (0)
      AROWS();
      if (__builtin_expect(__any(sq != sq), 0)) {
#pragma unroll 1
        for (int tr = 0; tr < RETRY && !fresh(umax4(xv[3], umax4(xv[2], umax4(xv[1], umax4(xv[0], 0u))))); tr++) {
          __builtin_amdgcn_s_sleep(XSLEEP);
          asm volatile("" ::: "memory");
#pragma unroll
          for (int i = 0; i < 4; i++) xv[i] = bld1(rw, row * 1024 + c16 * 16 + i * 256, WS_X2 + T * 32768);
        }
        AROWS();
      }
      sum += dppf<0xB1>(sum); sq += dppf<0xB1>(sq); sum += dppf<0x4E>(sum); sq += dppf<0x4E>(sq);
      sum += dppf<0x141>(sum); sq += dppf<0x141>(sq); sum += dppf<0x140>(sum); sq += dppf<0x140>(sq);
      float mean, rinv; ln_scalars(sum, sq, mean, rinv);
      if (c16 < 2) *(LAS float*)(sm + 32768 + c16 * 128 + row * 4) = c16 ? rinv : -mean;
      asm volatile("s_waitcnt lgkmcnt(0)\n\ts_barrier" ::: "memory");
      const int kg = l >> 4;
      f32x4 a0 = {}, a1 = {}, aj = {};
      const char* xq = sm + kg * 256;
      const char* stj = sm + 32768 + (j & 1) * 64; const char* stt = sm + 32768 + ((j & 1) ^ 1) * 64;
      const float nm0 = *(const LAS float*)(stj + (l & 15) * 4), ri0 = *(const LAS float*)(stj + 128 + (l & 15) * 4);
      const float nm1 = *(const LAS float*)(stt + (l & 15) * 4), ri1 = *(const LAS float*)(stt + 128 + (l & 15) * 4);
      const char* xj = xq + (j & 1) * 16384; const char* xt = xq + ((j & 1) ^ 1) * 16384;
      u32x4 fj[2][4], ft[2][4];
#define ARD(g) _Pragma("unroll") for (int u = 0; u < 4; u++) { const int so = ((g) * 4 + u) * 1024 + (((l & 15) ^ (4 * (u & 1) + kg)) * 16); fj[(g) & 1][u] = lds128(xj + so); ft[(g) & 1][u] = lds128(xt + so); }
#define ALOOP(JOB, TAG) do { asm volatile("; " TAG ::: "memory"); ARD(0); _Pragma("unroll") for (int g = 0; g < 4; g++) { if (g < 3) { asm volatile("; " TAG ::: "memory"); ARD(g + 1); } SBAR(); \
        _Pragma("unroll") for (int u = 0; u < 4; u++) { const int ks = g * 4 + u; JOB; a0 = mfma16(WQ[ks], fj[g & 1][u], a0); a1 = mfma16(WQ[ks], ft[g & 1][u], a1); } SBAR(); } } while (0)
      if (w != JW) ALOOP((void)0, "q");
      else if (isV) ALOOP(aj = mfma16(fj[g & 1][u], WJ[ks], aj), "v");
      else ALOOP(aj = mfma16(WJ[ks], fj[g & 1][u], aj), "k");
      if (w == JW) {
        if (isV) aj = (aj + *(const LAS f32x4*)(stj + kg * 16) * cj[0]) * *(const LAS f32x4*)(stj + 128 + kg * 16);
        else aj = (aj + nm0 * cj) * ri0;
        unsigned e0 = pack2(aj[0], aj[1]), e1 = pack2(aj[2], aj[3]), f0 = e0, f1 = e1;
        sw16(e0, f0); sw16(e1, f1);
        if ((l & 16) == 0) bst1(u32x4{e0, e1, f0, f1}, rw, ((l >> 5) * 32 + (l & 15)) * 16, nl + (isV ? L_V : L_K) + T * 1024 + (j & 1) * 256);
        if (!isV) {
          float n = aj[0] * aj[0] + aj[1] * aj[1] + aj[2] * aj[2] + aj[3] * aj[3];
          const unsigned nk = row_umax(__float_as_uint(add32(add16(n))));
          if (l < 8) bst1(u32x4{nk, nk, nk, nk}, rw, l * 16, nl + L_KN + (T * 2 + (j & 1)) * 128);
        }
      }
      a0 = (a0 + nm0 * cs) * (ri0 * QSCALE); a1 = (a1 + nm1 * cs) * (ri1 * QSCALE);
      unsigned p0 = pack2(a0[0], a0[1]), p1 = pack2(a0[2], a0[3]), q0 = pack2(a1[0], a1[1]), q1 = pack2(a1[2], a1[3]);
      sw16(p0, q0); sw16(p1, q1);
      bst1(u32x4{p0, p1, q0, q1}, rw, (((kg ^ j) & 1) * 2 + ((l >> 3) & 1)) * 8192 + ((kg >> 1) * 32 + (l & 7)) * 16, nl + L_QF + T * 32768 + (head >> 2) * 1024 + (head & 3) * 128);
      asm volatile("s_waitcnt vmcnt(0)\n\ts_barrier\nk_main_code_end:" ::: "memory");
      if (w == 2 && l < 8) flag_add(rw, FL_G + (l * 8 + (lin >> 5)) * 256);
    }
  }
}

extern "C" void kernel_launch(void* const* d_in, const int* in_sizes, int n_in,
                              void* d_out, int out_size, void* d_ws, size_t ws_size,
                              hipStream_t stream) {
  const u16* x = (const u16*)d_in[0];
  char* ws = (char*)d_ws;
  k_pre<<<256, 832, 0, stream>>>(x, (const u16*)d_in[1], (const u16*)d_in[2], (const u16*)d_in[3], (const u16*)d_in[4],
                                 (const u16*)d_in[6], (const u16*)d_in[7], (const u16*)d_in[8], ws);
  k_main<<<256, 512, 0, stream>>>(x, ws, (u16*)d_out);
}
```
